```python
import torch

def _softmax(x, axis=-1):
    return torch.softmax(x, dim=axis)
def _gelu(x):
    return torch.nn.functional.gelu(x, approximate="tanh")
def _silu(x):
    return torch.nn.functional.silu(x)
def _sigmoid(x):
    return torch.sigmoid(x)
def _layernorm(x, axis=-1, eps=1e-5):
    m = x.mean(dim=axis, keepdim=True)
    v = x.var(dim=axis, keepdim=True, unbiased=False)
    return (x - m) / torch.sqrt(v + eps)
def _scatter_add(tgt, idx, src, axis=0):
    return tgt.clone().index_add_(axis, idx, src)
def _segment_sum(src, seg, n):
    out = torch.zeros((n,) + src.shape[1:], dtype=src.dtype)
    return out.index_add_(0, seg, src)
def _pad(x, pw):
    flat = tuple(v for pair in reversed(pw) for v in pair)
    return torch.nn.functional.pad(x, flat)

kernel_name = 'gather_scatter__edge_featured_mpnn_mpnn__graph'

def setup_inputs():

    _w = (lambda a: (lambda t: t.to(torch.bfloat16) if t.is_floating_point() else t)(torch.as_tensor(a)))
    N, deg, D, De = 10000, 32, 128, max(0, 1)
    E = N * deg
    H = 2 * D
    return {
        "x": _w((lambda *s: torch.randn(*s, dtype=torch.bfloat16) * 0.05)(N, D)),
        "e_attr": _w((lambda *s: torch.randn(*s, dtype=torch.bfloat16) * 1.6)(E, De)),
        "src": _w((lambda lo, hi, *s: torch.randint(lo, hi, s))(0, N, E).to(torch.int64)),
        "dst": _w((lambda lo, hi, *s: torch.randint(lo, hi, s))(0, N, E).to(torch.int64)),
        "w1": _w((lambda *s: torch.randn(*s, dtype=torch.bfloat16) * 1.6)(2 * D + De, H) * (1.0 / (2 * D + De) ** 0.5)),
        "b1": _w((lambda *s: torch.randn(*s, dtype=torch.bfloat16) * 0.8)(H) * 0.01),
        "w2": _w((lambda *s: torch.randn(*s, dtype=torch.bfloat16) * 0.05)(H, D) * (1.0 / H ** 0.5)),
        "b2": _w((lambda *s: torch.randn(*s, dtype=torch.bfloat16) * 0.05)(D) * 0.01),
    }

def reference(x, e_attr, src, dst, w1, b1, w2, b2):
    N, D = x.shape
    h_src = x[src]
    h_dst = x[dst]
    m_in = torch.concatenate([h_src, h_dst, e_attr], axis=-1)
    h = _silu(m_in @ w1 + b1)
    msg = h @ w2 + b2
    return _segment_sum(msg, dst, N)
```

```cpp
#include <hip/hip_runtime.h>
#include <hip/hip_bf16.h>
#include <stdint.h>

typedef __attribute__((ext_vector_type(8))) __bf16 bf16x8;
typedef __attribute__((ext_vector_type(4))) float f32x4;

static constexpr int D = 128;
static constexpr int H = 256;
static constexpr int NPB = 20;
static constexpr int NCHUNK = 128;
static constexpr int CAP = 1024;
static constexpr int MAIN_THREADS = 512;
static constexpr int MAIN_WAVES = MAIN_THREADS / 64;
static constexpr int SROW = 260;
static constexpr float NEG_LOG2E = -1.4426950408889634f;
static constexpr float NEG_LN2 = -0.6931471805599453f;

__device__ __forceinline__ float bf16_bits_to_f32(uint32_t b) { return __uint_as_float(b << 16); }

__device__ __forceinline__ uint16_t f32_to_bf16_bits(float f) {
    __hip_bfloat16 h = __float2bfloat16(f);
    return *reinterpret_cast<uint16_t*>(&h);
}

static constexpr int PRE_THREADS = 256;
static constexpr int WT_STRIDE = 136;

__global__ __launch_bounds__(PRE_THREADS) void prep_kernel(
    const uint16_t* __restrict__ x, const uint16_t* __restrict__ e_attr,
    const int* __restrict__ src, const int* __restrict__ dst,
    const uint16_t* __restrict__ w1, const uint16_t* __restrict__ b1,
    const uint16_t* __restrict__ w2,
    uint16_t* __restrict__ P, float* __restrict__ Q, uint16_t* __restrict__ w2f,
    int2* __restrict__ ent, int* __restrict__ offs,
    int N, int E, int NB, int chunk_len, int n_gemm_blocks)
{
    __shared__ __attribute__((aligned(16))) unsigned char smem[128 * WT_STRIDE * 2];
    const int tid = threadIdx.x;
    const int bid = blockIdx.x;

    if (bid < NCHUNK) {
        int* cnt = reinterpret_cast<int*>(smem);
        int* wsum = cnt + 4096;
        for (int i = tid; i <= NB; i += PRE_THREADS) cnt[i] = 0;
        __syncthreads();
        const int e0 = bid * chunk_len;
        const int e1 = min(E, e0 + chunk_len);
        constexpr int MAXPT = 16;
        int kk[MAXPT], rr[MAXPT];
#pragma unroll
        for (int j = 0; j < MAXPT; ++j) {
            int e = e0 + tid + j * PRE_THREADS;
            kk[j] = -1; rr[j] = 0;
            if (e < e1) {
                int d = dst[e];
                int k = d / NPB;
                kk[j] = k;
                rr[j] = atomicAdd(&cnt[k], 1);
            }
        }
        __syncthreads();
        const int per = (NB + PRE_THREADS - 1) / PRE_THREADS;
        const int i0 = tid * per;
        int local = 0;
        for (int i = i0; i < min(NB, i0 + per); ++i) local += cnt[i];
        int incl = local;
        const int lane = tid & 63, wv = tid >> 6;
#pragma unroll
        for (int s = 1; s < 64; s <<= 1) {
            int t = __shfl_up(incl, s, 64);
            if (lane >= s) incl += t;
        }
        if (lane == 63) wsum[wv] = incl;
        __syncthreads();
        int base = 0;
        for (int w = 0; w < wv; ++w) base += wsum[w];
        int run = base + incl - local;
        for (int i = i0; i < min(NB, i0 + per); ++i) { int c = cnt[i]; cnt[i] = run; run += c; }
        __syncthreads();
        if (tid == 0) cnt[NB] = e1 > e0 ? e1 - e0 : 0;
        __syncthreads();
        for (int i = tid; i <= NB; i += PRE_THREADS) offs[(size_t)bid * (NB + 1) + i] = cnt[i];
#pragma unroll
        for (int j = 0; j < MAXPT; ++j) {
            int e = e0 + tid + j * PRE_THREADS;
            if (e < e1) {
                int k = kk[j];
                int d = dst[e];
                int pos = e0 + cnt[k] + rr[j];
                int2 v;
                v.x = src[e];
                v.y = ((d - k * NPB) << 16) | (int)e_attr[e];
                ent[pos] = v;
            }
        }
        return;
    }
    if (bid < NCHUNK + n_gemm_blocks) {
        const int g = bid - NCHUNK;
        const int cb = g & 3;
        const int rb = g >> 2;
        const int krow0 = (cb >> 1) * D;
        const int ccol0 = (cb & 1) * 128;
        uint16_t* wt = reinterpret_cast<uint16_t*>(smem);
        for (int i = tid; i < 128 * 64; i += PRE_THREADS) {
            int k = i >> 6;
            int c2 = (i & 63) * 2;
            uint32_t v = *reinterpret_cast<const uint32_t*>(w1 + (size_t)(krow0 + k) * H + ccol0 + c2);
            wt[(c2) * WT_STRIDE + k] = (uint16_t)(v & 0xffff);
            wt[(c2 + 1) * WT_STRIDE + k] = (uint16_t)(v >> 16);
        }
        __syncthreads();
        const int lane = tid & 63, wv = tid >> 6;
        const int node = rb * 64 + wv * 16 + (lane & 15);
        const int nodec = min(node, N - 1);
        bf16x8 bfr[4];
#pragma unroll
        for (int ks = 0; ks < 4; ++ks) {
            bfr[ks] = *reinterpret_cast<const bf16x8*>(x + (size_t)nodec * D + ks * 32 + (lane >> 4) * 8);
        }
#pragma unroll
        for (int ct = 0; ct < 8; ++ct) {
            f32x4 acc = {0.f, 0.f, 0.f, 0.f};
#pragma unroll
            for (int ks = 0; ks < 4; ++ks) {
                bf16x8 afr = *reinterpret_cast<const bf16x8*>(wt + (ct * 16 + (lane & 15)) * WT_STRIDE + ks * 32 + (lane >> 4) * 8);
                acc = __builtin_amdgcn_mfma_f32_16x16x32_bf16(afr, bfr[ks], acc, 0, 0, 0);
            }
            const int c = ccol0 + ct * 16 + (lane >> 4) * 4;
            if (node < N) {
                if (cb < 2) {
                    uint32_t lo = (uint32_t)f32_to_bf16_bits(acc[0] * NEG_LOG2E) | ((uint32_t)f32_to_bf16_bits(acc[1] * NEG_LOG2E) << 16);
                    uint32_t hi = (uint32_t)f32_to_bf16_bits(acc[2] * NEG_LOG2E) | ((uint32_t)f32_to_bf16_bits(acc[3] * NEG_LOG2E) << 16);
                    uint2 v; v.x = lo; v.y = hi;
                    *reinterpret_cast<uint2*>(P + (size_t)node * H + c) = v;
                } else {
                    float4 v;
                    v.x = (acc[0] + bf16_bits_to_f32(b1[c + 0])) * NEG_LOG2E;
                    v.y = (acc[1] + bf16_bits_to_f32(b1[c + 1])) * NEG_LOG2E;
                    v.z = (acc[2] + bf16_bits_to_f32(b1[c + 2])) * NEG_LOG2E;
                    v.w = (acc[3] + bf16_bits_to_f32(b1[c + 3])) * NEG_LOG2E;
                    *reinterpret_cast<float4*>(Q + (size_t)node * H + c) = v;
                }
            }
        }
        return;
    }
    for (int i = tid; i < H * D; i += PRE_THREADS) {
        int j = i & 7;
        int lane = (i >> 3) & 63;
        int ks = (i >> 9) & 7;
        int ct = i >> 12;
        int k = ks * 32 + (lane >> 4) * 8 + j;
        int c = ct * 16 + (lane & 15);
        w2f[i] = w2[(size_t)k * D + c];
    }
}

__device__ __forceinline__ void silu_acc(float& acc, float u) {
    float t = __builtin_amdgcn_exp2f(u);
    float r = __builtin_amdgcn_rcpf(1.0f + t);
    acc = __builtin_fmaf(u, r, acc);
}

__global__ __launch_bounds__(MAIN_THREADS) void main_kernel(
    const uint16_t* __restrict__ P, const float* __restrict__ Q,
    const uint16_t* __restrict__ w1, const uint16_t* __restrict__ w2f,
    const uint16_t* __restrict__ b2,
    const int2* __restrict__ ent, const int* __restrict__ offs,
    uint16_t* __restrict__ out, int N, int NB, int chunk_len)
{
    __shared__ __attribute__((aligned(16))) float sS[32 * SROW];
    __shared__ __attribute__((aligned(16))) float sQ[NPB * H];
    __shared__ __attribute__((aligned(16))) int2 sEnt[CAP];
    __shared__ int sCstart[NCHUNK];
    __shared__ int sCpre[NCHUNK + 1];
    __shared__ int sNcnt[32];
    __shared__ int sNoff[33];
    __shared__ int sDeg[32];

    const int tid = threadIdx.x;
    const int lane = tid & 63;
    const int wv = tid >> 6;
    const int k = blockIdx.x;
    const int n0 = k * NPB;
    const int nn = min(NPB, N - n0);

    if (tid < NCHUNK) {
        int s0 = offs[(size_t)tid * (NB + 1) + k];
        int s1 = offs[(size_t)tid * (NB + 1) + k + 1];
        sCstart[tid] = tid * chunk_len + s0;
        sCpre[tid + 1] = s1 - s0;
    }
    for (int i = tid; i < 32 * SROW; i += MAIN_THREADS) sS[i] = 0.f;
    for (int i = tid; i < nn * (H / 4); i += MAIN_THREADS) {
        reinterpret_cast<float4*>(sQ)[i] = reinterpret_cast<const float4*>(Q + (size_t)n0 * H)[i];
    }
    if (tid < 32) sDeg[tid] = 0;
    __syncthreads();
    if (wv == 0) {
        int a = sCpre[2 * lane + 1], b = sCpre[2 * lane + 2];
        int s = a + b;
        int incl = s;
#pragma unroll
        for (int d = 1; d < 64; d <<= 1) {
            int t = __shfl_up(incl, d, 64);
            if (lane >= d) incl += t;
        }
        int excl = incl - s;
        sCpre[2 * lane + 1] = excl + a;
        sCpre[2 * lane + 2] = excl + a + b;
        if (lane == 0) sCpre[0] = 0;
    }
    __syncthreads();
    const int total = sCpre[NCHUNK];

    float wq[4];
#pragma unroll
    for (int j = 0; j < 4; ++j) wq[j] = bf16_bits_to_f32(w1[(size_t)(2 * D) * H + lane * 4 + j]) * NEG_LOG2E;

    for (int p0 = 0; p0 < total; p0 += CAP) {
        const int cnt = min(CAP, total - p0);
        if (tid < 32) sNcnt[tid] = 0;
        __syncthreads();
        int2 ev[CAP / MAIN_THREADS];
        int er[CAP / MAIN_THREADS];
#pragma unroll
        for (int j = 0; j < CAP / MAIN_THREADS; ++j) {
            int i = tid + j * MAIN_THREADS;
            ev[j].x = 0; ev[j].y = 0; er[j] = 0;
            if (i < cnt) {
                int p = p0 + i;
                int lo = 0, hi = NCHUNK;
                while (hi - lo > 1) {
                    int mid = (lo + hi) >> 1;
                    if (sCpre[mid] <= p) lo = mid; else hi = mid;
                }
                ev[j] = ent[sCstart[lo] + (p - sCpre[lo])];
                er[j] = atomicAdd(&sNcnt[ev[j].y >> 16], 1);
            }
        }
        __syncthreads();
        if (tid == 0) {
            int run = 0;
            for (int i = 0; i < 32; ++i) { sNoff[i] = run; int c = sNcnt[i]; run += c; sDeg[i] += c; }
            sNoff[32] = run;
        }
        __syncthreads();
#pragma unroll
        for (int j = 0; j < CAP / MAIN_THREADS; ++j) {
            int i = tid + j * MAIN_THREADS;
            if (i < cnt) sEnt[sNoff[ev[j].y >> 16] + er[j]] = ev[j];
        }
        __syncthreads();

        const int per = (cnt + MAIN_WAVES - 1) / MAIN_WAVES;
        const int c0 = wv * per;
        const int c1 = min(cnt, c0 + per);
        int cur = -1;
        float q[4] = {0.f, 0.f, 0.f, 0.f};
        float acc[4] = {0.f, 0.f, 0.f, 0.f};
        for (int base = c0; base < c1; base += 64) {
            const int m = min(64, c1 - base);
            int2 my = sEnt[min(base + lane, c1 - 1)];
            int i = 0;
            for (; i + 4 <= m; i += 4) {
                uint2 pv[4];
                int yy[4];
#pragma unroll
                for (int u = 0; u < 4; ++u) {
                    int s = __builtin_amdgcn_readlane(my.x, i + u);
                    yy[u] = __builtin_amdgcn_readlane(my.y, i + u);
                    pv[u] = *reinterpret_cast<const uint2*>(P + (size_t)s * H + lane * 4);
                }
#pragma unroll
                for (int u = 0; u < 4; ++u) {
                    int node = yy[u] >> 16;
                    if (node != cur) {
                        if (cur >= 0) {
#pragma unroll
                            for (int j = 0; j < 4; ++j) { atomicAdd(&sS[cur * SROW + lane * 4 + j], acc[j]); acc[j] = 0.f; }
                        }
                        float4 qq = *reinterpret_cast<const float4*>(sQ + node * H + lane * 4);
                        q[0] = qq.x; q[1] = qq.y; q[2] = qq.z; q[3] = qq.w;
                        cur = node;
                    }
                    float e = bf16_bits_to_f32((uint32_t)yy[u] & 0xffffu);
                    float p0f = __uint_as_float(pv[u].x << 16);
                    float p1f = __uint_as_float(pv[u].x & 0xffff0000u);
                    float p2f = __uint_as_float(pv[u].y << 16);
                    float p3f = __uint_as_float(pv[u].y & 0xffff0000u);
                    silu_acc(acc[0], __builtin_fmaf(e, wq[0], q[0]) + p0f);
                    silu_acc(acc[1], __builtin_fmaf(e, wq[1], q[1]) + p1f);
                    silu_acc(acc[2], __builtin_fmaf(e, wq[2], q[2]) + p2f);
                    silu_acc(acc[3], __builtin_fmaf(e, wq[3], q[3]) + p3f);
                }
            }
            for (; i < m; ++i) {
                int s = __builtin_amdgcn_readlane(my.x, i);
                int y = __builtin_amdgcn_readlane(my.y, i);
                uint2 pv = *reinterpret_cast<const uint2*>(P + (size_t)s * H + lane * 4);
                int node = y >> 16;
                if (node != cur) {
                    if (cur >= 0) {
#pragma unroll
                        for (int j = 0; j < 4; ++j) { atomicAdd(&sS[cur * SROW + lane * 4 + j], acc[j]); acc[j] = 0.f; }
                    }
                    float4 qq = *reinterpret_cast<const float4*>(sQ + node * H + lane * 4);
                    q[0] = qq.x; q[1] = qq.y; q[2] = qq.z; q[3] = qq.w;
                    cur = node;
                }
                float e = bf16_bits_to_f32((uint32_t)y & 0xffffu);
                float p0f = __uint_as_float(pv.x << 16);
                float p1f = __uint_as_float(pv.x & 0xffff0000u);
                float p2f = __uint_as_float(pv.y << 16);
                float p3f = __uint_as_float(pv.y & 0xffff0000u);
                silu_acc(acc[0], __builtin_fmaf(e, wq[0], q[0]) + p0f);
                silu_acc(acc[1], __builtin_fmaf(e, wq[1], q[1]) + p1f);
                silu_acc(acc[2], __builtin_fmaf(e, wq[2], q[2]) + p2f);
                silu_acc(acc[3], __builtin_fmaf(e, wq[3], q[3]) + p3f);
            }
        }
        if (cur >= 0) {
#pragma unroll
            for (int j = 0; j < 4; ++j) atomicAdd(&sS[cur * SROW + lane * 4 + j], acc[j]);
        }
        __syncthreads();
    }

    {
        const int ct = wv;
        bf16x8 afr[8];
#pragma unroll
        for (int ks = 0; ks < 8; ++ks) {
            afr[ks] = *reinterpret_cast<const bf16x8*>(w2f + ((size_t)(ct * 8 + ks) * 64 + lane) * 8);
        }
        const int cc = ct * 16 + (lane >> 4) * 4;
        float bb[4];
#pragma unroll
        for (int j = 0; j < 4; ++j) bb[j] = bf16_bits_to_f32(b2[cc + j]);
#pragma unroll
        for (int nt = 0; nt < 2; ++nt) {
            const int nl = nt * 16 + (lane & 15);
            f32x4 acc = {0.f, 0.f, 0.f, 0.f};
#pragma unroll
            for (int ks = 0; ks < 8; ++ks) {
                const float* sp = sS + nl * SROW + ks * 32 + (lane >> 4) * 8;
                float4 v0 = *reinterpret_cast<const float4*>(sp);
                float4 v1 = *reinterpret_cast<const float4*>(sp + 4);
                float f[8] = {v0.x, v0.y, v0.z, v0.w, v1.x, v1.y, v1.z, v1.w};
                bf16x8 hi, lo;
#pragma unroll
                for (int j = 0; j < 8; ++j) {
                    float s = f[j] * NEG_LN2;
                    __bf16 h = (__bf16)s;
                    hi[j] = h;
                    lo[j] = (__bf16)(s - (float)h);
                }
                acc = __builtin_amdgcn_mfma_f32_16x16x32_bf16(afr[ks], hi, acc, 0, 0, 0);
                acc = __builtin_amdgcn_mfma_f32_16x16x32_bf16(afr[ks], lo, acc, 0, 0, 0);
            }
            if (nl < nn) {
                float dg = (float)sDeg[nl];
                uint32_t o0 = (uint32_t)f32_to_bf16_bits(acc[0] + dg * bb[0]) | ((uint32_t)f32_to_bf16_bits(acc[1] + dg * bb[1]) << 16);
                uint32_t o1 = (uint32_t)f32_to_bf16_bits(acc[2] + dg * bb[2]) | ((uint32_t)f32_to_bf16_bits(acc[3] + dg * bb[3]) << 16);
                uint2 v; v.x = o0; v.y = o1;
                *reinterpret_cast<uint2*>(out + (size_t)(n0 + nl) * D + cc) = v;
            }
        }
    }
}

static inline size_t align_up(size_t v, size_t a) { return (v + a - 1) / a * a; }

extern "C" void kernel_launch(void* const* d_in, const int* in_sizes, int n_in,
                              void* d_out, int out_size, void* d_ws, size_t ws_size,
                              hipStream_t stream) {
    const uint16_t* x = (const uint16_t*)d_in[0];
    const uint16_t* e_attr = (const uint16_t*)d_in[1];
    const int* src = (const int*)d_in[2];
    const int* dst = (const int*)d_in[3];
    const uint16_t* w1 = (const uint16_t*)d_in[4];
    const uint16_t* b1 = (const uint16_t*)d_in[5];
    const uint16_t* w2 = (const uint16_t*)d_in[6];
    const uint16_t* b2 = (const uint16_t*)d_in[7];
    uint16_t* out = (uint16_t*)d_out;

    const int N = in_sizes[0] / D;
    const int E = in_sizes[2];
    const int NB = (N + NPB - 1) / NPB;
    const int chunk_len = (E + NCHUNK - 1) / NCHUNK;
    const int n_gemm_blocks = ((N + 63) / 64) * 4;

    unsigned char* ws = (unsigned char*)d_ws;
    size_t off = 0;
    uint16_t* P = (uint16_t*)(ws + off); off = align_up(off + (size_t)N * H * 2, 256);
    float* Q = (float*)(ws + off); off = align_up(off + (size_t)N * H * 4, 256);
    uint16_t* w2f = (uint16_t*)(ws + off); off = align_up(off + (size_t)H * D * 2, 256);
    int2* ent = (int2*)(ws + off); off = align_up(off + (size_t)NCHUNK * chunk_len * 8, 256);
    int* offs = (int*)(ws + off); off = align_up(off + (size_t)NCHUNK * (NB + 1) * 4, 256);

    prep_kernel<<<NCHUNK + n_gemm_blocks + 1, PRE_THREADS, 0, stream>>>(
        x, e_attr, src, dst, w1, b1, w2, P, Q, w2f, ent, offs, N, E, NB, chunk_len, n_gemm_blocks);
    main_kernel<<<NB, MAIN_THREADS, 0, stream>>>(P, Q, w1, w2f, b2, ent, offs, out, N, NB, chunk_len);
}
```

```cpp
#include <hip/hip_runtime.h>
#include <hip/hip_bf16.h>
#include <stdint.h>

typedef __attribute__((ext_vector_type(8))) __bf16 bf16x8;
typedef __attribute__((ext_vector_type(4))) float f32x4;

#define STORE_COND
#define FLUSH(p, v) atomicAdd(p, v)
static constexpr int D = 128;
static constexpr int H = 256;
static constexpr int NPB = 20;
static constexpr int NCHUNK = 128;
static constexpr int CAP = 1024;
static constexpr int MAIN_THREADS = 512;
static constexpr int MAIN_WAVES = MAIN_THREADS / 64;
static constexpr int GRP = 4;
static constexpr int SROW = 260;
static constexpr float NEG_LOG2E = -1.4426950408889634f;
static constexpr float NEG_LN2 = -0.6931471805599453f;

__device__ __forceinline__ float bf16_bits_to_f32(uint32_t b) { return __uint_as_float(b << 16); }

__device__ __forceinline__ uint16_t f32_to_bf16_bits(float f) {
    __hip_bfloat16 h = __float2bfloat16(f);
    return *reinterpret_cast<uint16_t*>(&h);
}

__device__ __forceinline__ uint32_t pack_pair(float lo, float hi) {
    uint32_t l16 = f32_to_bf16_bits(lo);
    uint32_t bits = __float_as_uint(hi);
    uint32_t sign = bits & 0x80000000u;
    uint32_t mag = bits & 0x7fffffffu;
    uint32_t d = mag - l16;
    uint32_t h = min((d + 0x8000u) >> 16, 0x7f7fu);
    h = mag < l16 ? 0u : h;
    h = mag >= 0x7f800000u ? (mag >> 16) : h;
    return sign | (h << 16) | l16;
}

static constexpr int PRE_THREADS = 512;
static constexpr int ST_STRIDE = 132;
static constexpr int WT_STRIDE = 136;

__global__ __launch_bounds__(PRE_THREADS) void prep_kernel(
    const uint16_t* __restrict__ x, const uint16_t* __restrict__ e_attr,
    const int* __restrict__ src, const int* __restrict__ dst,
    const uint16_t* __restrict__ w1, const uint16_t* __restrict__ b1,
    const uint16_t* __restrict__ w2,
    uint16_t* __restrict__ P, float* __restrict__ Q, uint16_t* __restrict__ w2f,
    int2* __restrict__ ent, int* __restrict__ offs,
    int N, int E, int NB, int chunk_len, int n_gemm_blocks)
{
    __shared__ __attribute__((aligned(16))) unsigned char smem[8 * 16 * ST_STRIDE * 4];
    const int tid = threadIdx.x;
    const int bid = blockIdx.x;

    if (bid < NCHUNK) {
        int* cnt = reinterpret_cast<int*>(smem);
        int* wsum = cnt + 4096;
        for (int i = tid; i <= NB; i += PRE_THREADS) cnt[i] = 0;
        __syncthreads();
        const int e0 = bid * chunk_len;
        const int e1 = min(E, e0 + chunk_len);
        constexpr int MAXPT = 8;
        int dd[MAXPT], rr[MAXPT];
#pragma unroll
        for (int j = 0; j < MAXPT; ++j) {
            int e = e0 + tid + j * PRE_THREADS;
            dd[j] = dst[min(e, E - 1)];
        }
#pragma unroll
        for (int j = 0; j < MAXPT; ++j) {
            int e = e0 + tid + j * PRE_THREADS;
            rr[j] = 0;
            if (e < e1) rr[j] = atomicAdd(&cnt[dd[j] / NPB], 1);
        }
        int sv[MAXPT];
        uint16_t av[MAXPT];
#pragma unroll
        for (int j = 0; j < MAXPT; ++j) {
            int e = min(e0 + tid + j * PRE_THREADS, E - 1);
            sv[j] = src[e];
            av[j] = e_attr[e];
        }
        __syncthreads();
        const int per = (NB + PRE_THREADS - 1) / PRE_THREADS;
        const int i0 = tid * per;
        int local = 0;
        for (int i = i0; i < min(NB, i0 + per); ++i) local += cnt[i];
        int incl = local;
        const int lane = tid & 63, wv = tid >> 6;
#pragma unroll
        for (int s = 1; s < 64; s <<= 1) {
            int t = __shfl_up(incl, s, 64);
            if (lane >= s) incl += t;
        }
        if (lane == 63) wsum[wv] = incl;
        __syncthreads();
        int base = 0;
        for (int w = 0; w < wv; ++w) base += wsum[w];
        int run = base + incl - local;
        for (int i = i0; i < min(NB, i0 + per); ++i) { int c = cnt[i]; cnt[i] = run; run += c; }
        __syncthreads();
        if (tid == 0) cnt[NB] = e1 > e0 ? e1 - e0 : 0;
        __syncthreads();
        for (int i = tid; i <= NB; i += PRE_THREADS) offs[(size_t)bid * (NB + 1) + i] = cnt[i];
#pragma unroll
        for (int j = 0; j < MAXPT; ++j) {
            int e = e0 + tid + j * PRE_THREADS;
            if (e < e1) {
                int k = dd[j] / NPB;
                int pos = e0 + cnt[k] + rr[j];
                int2 v;
                v.x = sv[j];
                v.y = ((dd[j] - k * NPB) << 16) | (int)av[j];
                ent[pos] = v;
            }
        }
        return;
    }
    if (bid < NCHUNK + n_gemm_blocks) {
        const int g = bid - NCHUNK;
        const int cb = g & 3;
        const int rb = g >> 2;
        const int krow0 = (cb >> 1) * D;
        const int ccol0 = (cb & 1) * 128;
        const int lane = tid & 63, wv = tid >> 6;
        uint16_t* wt = reinterpret_cast<uint16_t*>(smem);
        bf16x8 bfr[1][4];
        {
            const int nodec = min(rb * 128 + wv * 16 + (lane & 15), N - 1);
#pragma unroll
            for (int ks = 0; ks < 4; ++ks)
                bfr[0][ks] = *reinterpret_cast<const bf16x8*>(x + (size_t)nodec * D + ks * 32 + (lane >> 4) * 8);
        }
        uint2 bv[8];
#pragma unroll
        for (int ct = 0; ct < 8; ++ct)
            bv[ct] = *reinterpret_cast<const uint2*>(b1 + ccol0 + ct * 16 + (lane >> 4) * 4);
        {
            uint32_t v[2][8];
#pragma unroll
            for (int r = 0; r < 2; ++r) {
                const int task = tid + r * PRE_THREADS;
                const int c2 = (task & 63) * 2;
                const int k0 = (task >> 6) * 8;
#pragma unroll
                for (int j = 0; j < 8; ++j)
                    v[r][j] = *reinterpret_cast<const uint32_t*>(w1 + (size_t)(krow0 + k0 + j) * H + ccol0 + c2);
            }
#pragma unroll
            for (int r = 0; r < 2; ++r) {
                const int task = tid + r * PRE_THREADS;
                const int c2 = (task & 63) * 2;
                const int k0 = (task >> 6) * 8;
                uint4 lo, hi;
                lo.x = (v[r][0] & 0xffffu) | (v[r][1] << 16);
                lo.y = (v[r][2] & 0xffffu) | (v[r][3] << 16);
                lo.z = (v[r][4] & 0xffffu) | (v[r][5] << 16);
                lo.w = (v[r][6] & 0xffffu) | (v[r][7] << 16);
                hi.x = (v[r][0] >> 16) | (v[r][1] & 0xffff0000u);
                hi.y = (v[r][2] >> 16) | (v[r][3] & 0xffff0000u);
                hi.z = (v[r][4] >> 16) | (v[r][5] & 0xffff0000u);
                hi.w = (v[r][6] >> 16) | (v[r][7] & 0xffff0000u);
                *reinterpret_cast<uint4*>(wt + c2 * WT_STRIDE + k0) = lo;
                *reinterpret_cast<uint4*>(wt + (c2 + 1) * WT_STRIDE + k0) = hi;
            }
        }
        __syncthreads();
        float* st = reinterpret_cast<float*>(smem) + wv * 16 * ST_STRIDE;
        {
            f32x4 acc[8];
#pragma unroll
            for (int ct = 0; ct < 8; ++ct) acc[ct] = f32x4{0.f, 0.f, 0.f, 0.f};
#pragma unroll
            for (int ks = 0; ks < 4; ++ks) {
#pragma unroll
                for (int ct = 0; ct < 8; ++ct) {
                    bf16x8 afr = *reinterpret_cast<const bf16x8*>(wt + (ct * 16 + (lane & 15)) * WT_STRIDE + ks * 32 + (lane >> 4) * 8);
                    acc[ct] = __builtin_amdgcn_mfma_f32_16x16x32_bf16(afr, bfr[0][ks], acc[ct], 0, 0, 0);
                }
            }
            __syncthreads();
            float* sp = st + (lane & 15) * ST_STRIDE;
            if (cb < 2) {
#pragma unroll
                for (int ct = 0; ct < 8; ++ct) {
                    uint2 v;
                    v.x = pack_pair(acc[ct][0] * NEG_LOG2E, acc[ct][1] * NEG_LOG2E);
                    v.y = pack_pair(acc[ct][2] * NEG_LOG2E, acc[ct][3] * NEG_LOG2E);
                    *reinterpret_cast<uint2*>(sp + ct * 8 + (lane >> 4) * 2) = v;
                }
            } else {
#pragma unroll
                for (int ct = 0; ct < 8; ++ct) {
                    float4 v;
                    v.x = (acc[ct][0] + __uint_as_float(bv[ct].x << 16)) * NEG_LOG2E;
                    v.y = (acc[ct][1] + __uint_as_float(bv[ct].x & 0xffff0000u)) * NEG_LOG2E;
                    v.z = (acc[ct][2] + __uint_as_float(bv[ct].y << 16)) * NEG_LOG2E;
                    v.w = (acc[ct][3] + __uint_as_float(bv[ct].y & 0xffff0000u)) * NEG_LOG2E;
                    *reinterpret_cast<float4*>(sp + ct * 16 + (lane >> 4) * 4) = v;
                }
            }
            __builtin_amdgcn_fence(__ATOMIC_RELEASE, "wavefront");
            __builtin_amdgcn_wave_barrier();
            const int nbase = rb * 128 + wv * 16;
            if (cb < 2) {
#pragma unroll
                for (int r = 0; r < 4; ++r) {
                    const int nl = r * 4 + (lane >> 4);
                    uint4 v = *reinterpret_cast<const uint4*>(st + nl * ST_STRIDE + (lane & 15) * 4);
                    if (nbase + nl < N STORE_COND) *reinterpret_cast<uint4*>(P + (size_t)(nbase + nl) * H + ccol0 + (lane & 15) * 8) = v;
                }
            } else {
#pragma unroll
                for (int r = 0; r < 8; ++r) {
                    const int nl = r * 2 + (lane >> 5);
                    uint4 v = *reinterpret_cast<const uint4*>(st + nl * ST_STRIDE + (lane & 31) * 4);
                    if (nbase + nl < N STORE_COND) *reinterpret_cast<uint4*>(Q + (size_t)(nbase + nl) * H + ccol0 + (lane & 31) * 4) = v;
                }
            }
            __builtin_amdgcn_fence(__ATOMIC_RELEASE, "wavefront");
            __builtin_amdgcn_wave_barrier();
        }
        return;
    }
    const int wb = bid - NCHUNK - n_gemm_blocks;
    if (wb == 0 && tid < H) P[(size_t)N * H + tid] = 0x7F7F;
#pragma unroll
    for (int r = 0; r < 2; ++r) {
        int i = wb * 1024 + r * PRE_THREADS + tid;
        int j = i & 7;
        int lane = (i >> 3) & 63;
        int ks = (i >> 9) & 7;
        int ct = i >> 12;
        int k = ks * 32 + (lane >> 4) * 8 + j;
        int c = ct * 16 + (lane & 15);
        w2f[i] = w2[(size_t)k * D + c];
    }
}

typedef __attribute__((ext_vector_type(2))) float f32x2;

__device__ __forceinline__ void edge4(f32x2& al, f32x2& ah, uint32_t px, uint32_t py, f32x2 ee,
                                      f32x2 wl, f32x2 wh, f32x2 ql, f32x2 qh) {
    f32x2 ul = __builtin_elementwise_fma(ee, wl, ql);
    f32x2 uh = __builtin_elementwise_fma(ee, wh, qh);
    f32x2 pl = {__uint_as_float(px << 16), __uint_as_float(py << 16)};
    f32x2 ph = {__uint_as_float(px), __uint_as_float(py)};
    ul += pl;
    uh += ph;
    f32x2 tl = {__builtin_amdgcn_exp2f(ul.x), __builtin_amdgcn_exp2f(ul.y)};
    f32x2 th = {__builtin_amdgcn_exp2f(uh.x), __builtin_amdgcn_exp2f(uh.y)};
    tl += 1.0f;
    th += 1.0f;
    f32x2 rl = {__builtin_amdgcn_rcpf(tl.x), __builtin_amdgcn_rcpf(tl.y)};
    f32x2 rh = {__builtin_amdgcn_rcpf(th.x), __builtin_amdgcn_rcpf(th.y)};
    al = __builtin_elementwise_fma(ul, rl, al);
    ah = __builtin_elementwise_fma(uh, rh, ah);
}

__global__ __launch_bounds__(MAIN_THREADS, 4) void main_kernel(
    const uint16_t* __restrict__ P, const float* __restrict__ Q,
    const uint16_t* __restrict__ w1, const uint16_t* __restrict__ w2f,
    const uint16_t* __restrict__ b2,
    const int2* __restrict__ ent, const int* __restrict__ offs,
    uint16_t* __restrict__ out, int N, int NB, int chunk_len)
{
    __shared__ __attribute__((aligned(16))) float sS[NPB * SROW];
    constexpr int ENT_CAP = CAP + 32 * GRP + 64;
    constexpr int HROW = H + 8;
    static_assert(2 * NPB * HROW * 2 <= NPB * H * 4 + ENT_CAP * 8, "hi/lo tiles must fit over sQ+sEnt");
    __shared__ __attribute__((aligned(16))) unsigned char sBuf[NPB * H * 4 + ENT_CAP * 8];
    float* sQ = reinterpret_cast<float*>(sBuf);
    int2* sEnt = reinterpret_cast<int2*>(sBuf + NPB * H * 4);
    __bf16* sHi = reinterpret_cast<__bf16*>(sBuf);
    __bf16* sLo = sHi + NPB * HROW;
    __shared__ __attribute__((aligned(16))) float sCarry[MAIN_WAVES * H];
    __shared__ int sCarryNode[MAIN_WAVES + 1];
    __shared__ int sCstart[NCHUNK];
    __shared__ int sCpre[NCHUNK + 1];
    __shared__ int sNcnt[32];
    __shared__ int sNoff[33];
    __shared__ int sDeg[32];

    const int tid = threadIdx.x;
    const int lane = tid & 63;
    const int wv = tid >> 6;
    const int k = blockIdx.x;
    const int n0 = k * NPB;
    const int nn = min(NPB, N - n0);

    if (tid < NCHUNK) {
        int s0 = offs[(size_t)tid * (NB + 1) + k];
        int s1 = offs[(size_t)tid * (NB + 1) + k + 1];
        sCstart[tid] = tid * chunk_len + s0;
        sCpre[tid + 1] = s1 - s0;
    }
    for (int i = tid; i < NPB * SROW; i += MAIN_THREADS) sS[i] = 0.f;
    static_assert(NPB * (H / 4) <= 3 * MAIN_THREADS, "Q staging assumes at most 3 float4 per thread");
    const int qn = nn * (H / 4);
    const float4* Qg = reinterpret_cast<const float4*>(Q + (size_t)n0 * H);
    const float4 qr0 = Qg[min(tid, qn - 1)];
    const float4 qr1 = Qg[min(tid + MAIN_THREADS, qn - 1)];
    const float4 qr2 = Qg[min(tid + 2 * MAIN_THREADS, qn - 1)];
    if (tid < 32) { sDeg[tid] = 0; sNcnt[tid] = 0; }
    if (tid <= MAIN_WAVES) sCarryNode[tid] = -1;
    __syncthreads();
    if (wv == 0) {
        int a = sCpre[2 * lane + 1], b = sCpre[2 * lane + 2];
        int s = a + b;
        int incl = s;
#pragma unroll
        for (int d = 1; d < 64; d <<= 1) {
            int t = __shfl_up(incl, d, 64);
            if (lane >= d) incl += t;
        }
        int excl = incl - s;
        sCpre[2 * lane + 1] = excl + a;
        sCpre[2 * lane + 2] = excl + a + b;
        if (lane == 0) sCpre[0] = 0;
    }
    __syncthreads();
    const int total = sCpre[NCHUNK];

    const int half = lane >> 5;
    const int sl = lane & 31;
    f32x2 wq[4];
    {
        uint4 wv4 = *reinterpret_cast<const uint4*>(w1 + (size_t)(2 * D) * H + sl * 8);
        wq[0] = f32x2{__uint_as_float(wv4.x << 16), __uint_as_float(wv4.y << 16)} * NEG_LOG2E;
        wq[1] = f32x2{__uint_as_float(wv4.x & 0xffff0000u), __uint_as_float(wv4.y & 0xffff0000u)} * NEG_LOG2E;
        wq[2] = f32x2{__uint_as_float(wv4.z << 16), __uint_as_float(wv4.w << 16)} * NEG_LOG2E;
        wq[3] = f32x2{__uint_as_float(wv4.z & 0xffff0000u), __uint_as_float(wv4.w & 0xffff0000u)} * NEG_LOG2E;
    }

    auto stage = [&](int p0, bool first) {
        const int cnt = min(CAP, total - p0);
        int2 ev[CAP / MAIN_THREADS];
        int er[CAP / MAIN_THREADS];
#pragma unroll
        for (int j = 0; j < CAP / MAIN_THREADS; ++j) {
            int p = p0 + min(tid + j * MAIN_THREADS, cnt - 1);
            int lo = 0;
#pragma unroll
            for (int stp = NCHUNK / 2; stp > 0; stp >>= 1) {
                if (sCpre[lo + stp] <= p) lo += stp;
            }
            ev[j] = ent[sCstart[lo] + (p - sCpre[lo])];
        }
        if (first) {
            float4* sQ4 = reinterpret_cast<float4*>(sQ);
            if (tid < qn) sQ4[tid] = qr0;
            if (tid + MAIN_THREADS < qn) sQ4[tid + MAIN_THREADS] = qr1;
            if (tid + 2 * MAIN_THREADS < qn) sQ4[tid + 2 * MAIN_THREADS] = qr2;
        }
#pragma unroll
        for (int j = 0; j < CAP / MAIN_THREADS; ++j) {
            er[j] = 0;
            if (tid + j * MAIN_THREADS < cnt) er[j] = atomicAdd(&sNcnt[ev[j].y >> 16], 1);
        }
        __syncthreads();
        if (wv == 0) {
            int c = lane < 32 ? sNcnt[lane] : 0;
            int cp = (c + GRP - 1) / GRP * GRP;
            int incl = cp;
#pragma unroll
            for (int d = 1; d < 32; d <<= 1) {
                int t = __shfl_up(incl, d, 64);
                if (lane >= d) incl += t;
            }
            if (lane < 32) {
                int st = incl - cp;
                sNoff[lane] = st;
                sDeg[lane] += c;
                if (lane == 31) sNoff[32] = incl;
                int2 pad; pad.x = N * (H * 2); pad.y = lane << 16;
                for (int i = c; i < cp; ++i) sEnt[st + i] = pad;
            }
        }
        __syncthreads();
#pragma unroll
        for (int j = 0; j < CAP / MAIN_THREADS; ++j) {
            int i = tid + j * MAIN_THREADS;
            if (i < cnt) sEnt[sNoff[ev[j].y >> 16] + er[j]] = make_int2(ev[j].x * (H * 2), ev[j].y);
        }
        __syncthreads();
    };

    auto merge = [&]() {
        const int nd = sCarryNode[wv];
        if (nd >= 0 && (wv == 0 || sCarryNode[wv - 1] != nd)) {
            float4 v = *reinterpret_cast<const float4*>(sS + nd * SROW + lane * 4);
            for (int r = wv; r < MAIN_WAVES && sCarryNode[r] == nd; ++r) {
                float4 cvv = *reinterpret_cast<const float4*>(sCarry + r * H + lane * 4);
                v.x += cvv.x; v.y += cvv.y; v.z += cvv.z; v.w += cvv.w;
            }
            *reinterpret_cast<float4*>(sS + nd * SROW + lane * 4) = v;
        }
    };

    if (total > 0) stage(0, true);
    for (int p0 = 0; p0 < total; p0 += CAP) {
        const int ngrp = sNoff[32] / GRP;
        const int gper = (ngrp + MAIN_WAVES - 1) / MAIN_WAVES;
        const int g0 = min(ngrp, wv * gper);
        const int c0 = __builtin_amdgcn_readfirstlane(g0 * GRP);
        const int n = __builtin_amdgcn_readfirstlane((min(ngrp, g0 + gper) - g0) * GRP);
        int cur = -1;
        int nseg = 0;
        f32x2 q[4], acc[4];
#pragma unroll
        for (int j = 0; j < 4; ++j) { q[j] = f32x2{0.f, 0.f}; acc[j] = f32x2{0.f, 0.f}; }
        if (n > 0) {
            auto flush = [&](int tgt) {
                float a8[8] = {acc[0].x, acc[1].x, acc[0].y, acc[1].y, acc[2].x, acc[3].x, acc[2].y, acc[3].y};
                float v[8];
#pragma unroll
                for (int j = 0; j < 8; ++j) v[j] = a8[j] + __shfl_xor(a8[j], 32, 64);
#pragma unroll
                for (int j = 0; j < 4; ++j) acc[j] = f32x2{0.f, 0.f};
                if (half == 0) {
                    float* dp;
                    if (tgt >= 0) {
                        dp = sS + tgt * SROW + sl * 8;
                        float4 o0 = *reinterpret_cast<const float4*>(dp);
                        float4 o1 = *reinterpret_cast<const float4*>(dp + 4);
                        v[0] += o0.x; v[1] += o0.y; v[2] += o0.z; v[3] += o0.w;
                        v[4] += o1.x; v[5] += o1.y; v[6] += o1.z; v[7] += o1.w;
                    } else {
                        dp = sCarry + wv * H + sl * 8;
                    }
                    *reinterpret_cast<float4*>(dp) = make_float4(v[0], v[1], v[2], v[3]);
                    *reinterpret_cast<float4*>(dp + 4) = make_float4(v[4], v[5], v[6], v[7]);
                }
                if (tgt < 0 && lane == 0) sCarryNode[wv] = cur;
            };
            constexpr int NS = 4;
            uint4 pvA[NS], pvB[NS];
            int yyA[NS], yyB[NS];
            const unsigned char* Pb = reinterpret_cast<const unsigned char*>(P);
            const uint32_t loff = sl * 16;
            const int2* sE = sEnt + c0 + half;
            const int last = n - 2;
            auto prefetch = [&](int i, uint4* pv, int* yy) {
#pragma unroll
                for (int u = 0; u < NS; ++u) {
                    int2 en = sE[min(i + 2 * u, last)];
                    yy[u] = en.y;
                    pv[u] = *reinterpret_cast<const uint4*>(Pb + ((uint32_t)en.x + loff));
                }
            };
            auto compute_group = [&](const uint4* pv, const int* yy) {
                int node = __builtin_amdgcn_readfirstlane(yy[0]) >> 16;
                if (node != cur) {
                    if (cur >= 0) {
                        flush(nseg == 0 ? -1 : cur);
                        ++nseg;
                    }
                    float4 qa = *reinterpret_cast<const float4*>(sQ + node * H + sl * 8);
                    float4 qb = *reinterpret_cast<const float4*>(sQ + node * H + sl * 8 + 4);
                    q[0] = f32x2{qa.x, qa.z}; q[1] = f32x2{qa.y, qa.w};
                    q[2] = f32x2{qb.x, qb.z}; q[3] = f32x2{qb.y, qb.w};
                    cur = node;
                }
#pragma unroll
                for (int u = 0; u < 2; ++u) {
                    float e = __uint_as_float((uint32_t)yy[u] << 16);
                    f32x2 ee = {e, e};
                    edge4(acc[0], acc[1], pv[u].x, pv[u].y, ee, wq[0], wq[1], q[0], q[1]);
                    edge4(acc[2], acc[3], pv[u].z, pv[u].w, ee, wq[2], wq[3], q[2], q[3]);
                }
            };
            prefetch(0, pvA, yyA);
            for (int i = 0; i < n; i += 4 * NS) {
                prefetch(i + 2 * NS, pvB, yyB);
                compute_group(pvA, yyA);
                if (i + GRP < n) compute_group(pvA + 2, yyA + 2);
                prefetch(i + 4 * NS, pvA, yyA);
                if (i + 2 * GRP < n) compute_group(pvB, yyB);
                if (i + 3 * GRP < n) compute_group(pvB + 2, yyB + 2);
            }
            flush(nseg == 0 ? -1 : cur);
        }
        if (p0 + CAP >= total) break;
        __syncthreads();
        merge();
        __syncthreads();
        if (tid < 32) sNcnt[tid] = 0;
        if (tid <= MAIN_WAVES) sCarryNode[tid] = -1;
        __syncthreads();
        stage(p0 + CAP, false);
    }

    bf16x8 afr[8];
#pragma unroll
    for (int ks = 0; ks < 8; ++ks)
        afr[ks] = *reinterpret_cast<const bf16x8*>(w2f + ((size_t)(wv * 8 + ks) * 64 + lane) * 8);
    const uint2 bbv = *reinterpret_cast<const uint2*>(b2 + wv * 16 + (lane >> 4) * 4);
    __syncthreads();
    merge();
    __syncthreads();
    for (int i = tid; i < NPB * (H / 2); i += MAIN_THREADS) {
        const int row = i / (H / 2), c2 = (i % (H / 2)) * 2;
        const float2 sv = *reinterpret_cast<const float2*>(sS + row * SROW + c2);
        const float s0 = sv.x * NEG_LN2, s1 = sv.y * NEG_LN2;
        const __bf16 h0 = (__bf16)s0, h1 = (__bf16)s1;
        const __bf16 l0 = (__bf16)(s0 - (float)h0), l1 = (__bf16)(s1 - (float)h1);
        typedef __attribute__((ext_vector_type(2))) __bf16 bf16x2;
        *reinterpret_cast<bf16x2*>(sHi + row * HROW + c2) = bf16x2{h0, h1};
        *reinterpret_cast<bf16x2*>(sLo + row * HROW + c2) = bf16x2{l0, l1};
    }
    __syncthreads();

    {
        const int cc = wv * 16 + (lane >> 4) * 4;
        float bb[4] = {__uint_as_float(bbv.x << 16), __uint_as_float(bbv.x & 0xffff0000u),
                       __uint_as_float(bbv.y << 16), __uint_as_float(bbv.y & 0xffff0000u)};
#pragma unroll
        for (int nt = 0; nt < 2; ++nt) {
            const int nl = nt * 16 + (lane & 15);
            const int row = min(nl, NPB - 1);
            f32x4 acc = {0.f, 0.f, 0.f, 0.f};
            {
#pragma unroll
                for (int ks = 0; ks < 8; ++ks) {
                    bf16x8 hi = *reinterpret_cast<const bf16x8*>(sHi + row * HROW + ks * 32 + (lane >> 4) * 8);
                    bf16x8 lo = *reinterpret_cast<const bf16x8*>(sLo + row * HROW + ks * 32 + (lane >> 4) * 8);
                    acc = __builtin_amdgcn_mfma_f32_16x16x32_bf16(afr[ks], hi, acc, 0, 0, 0);
                    acc = __builtin_amdgcn_mfma_f32_16x16x32_bf16(afr[ks], lo, acc, 0, 0, 0);
                }
            }
            if (nl < nn) {
                float dg = (float)sDeg[nl];
                uint32_t o0 = (uint32_t)f32_to_bf16_bits(acc[0] + dg * bb[0]) | ((uint32_t)f32_to_bf16_bits(acc[1] + dg * bb[1]) << 16);
                uint32_t o1 = (uint32_t)f32_to_bf16_bits(acc[2] + dg * bb[2]) | ((uint32_t)f32_to_bf16_bits(acc[3] + dg * bb[3]) << 16);
                uint2 v; v.x = o0; v.y = o1;
                *reinterpret_cast<uint2*>(out + (size_t)(n0 + nl) * D + cc) = v;
            }
        }
    }
}

static inline size_t align_up(size_t v, size_t a) { return (v + a - 1) / a * a; }

extern "C" void kernel_launch(void* const* d_in, const int* in_sizes, int n_in,
                              void* d_out, int out_size, void* d_ws, size_t ws_size,
                              hipStream_t stream) {
    const uint16_t* x = (const uint16_t*)d_in[0];
    const uint16_t* e_attr = (const uint16_t*)d_in[1];
    const int* src = (const int*)d_in[2];
    const int* dst = (const int*)d_in[3];
    const uint16_t* w1 = (const uint16_t*)d_in[4];
    const uint16_t* b1 = (const uint16_t*)d_in[5];
    const uint16_t* w2 = (const uint16_t*)d_in[6];
    const uint16_t* b2 = (const uint16_t*)d_in[7];
    uint16_t* out = (uint16_t*)d_out;

    const int N = in_sizes[0] / D;
    const int E = in_sizes[2];
    const int NB = (N + NPB - 1) / NPB;
    const int chunk_len = (E + NCHUNK - 1) / NCHUNK;
    const int n_gemm_blocks = ((N + 127) / 128) * 4;

    unsigned char* ws = (unsigned char*)d_ws;
    size_t off = 0;
    uint16_t* P = (uint16_t*)(ws + off); off = align_up(off + (size_t)(N + 1) * H * 2, 256);
    float* Q = (float*)(ws + off); off = align_up(off + (size_t)N * H * 4, 256);
    uint16_t* w2f = (uint16_t*)(ws + off); off = align_up(off + (size_t)H * D * 2, 256);
    int2* ent = (int2*)(ws + off); off = align_up(off + (size_t)NCHUNK * chunk_len * 8, 256);
    int* offs = (int*)(ws + off); off = align_up(off + (size_t)NCHUNK * (NB + 1) * 4, 256);

    prep_kernel<<<NCHUNK + n_gemm_blocks + 32, PRE_THREADS, 0, stream>>>(
        x, e_attr, src, dst, w1, b1, w2, P, Q, w2f, ent, offs, N, E, NB, chunk_len, n_gemm_blocks);
    main_kernel<<<NB, MAIN_THREADS, 0, stream>>>(P, Q, w1, w2f, b2, ent, offs, out, N, NB, chunk_len);
}
```

```cpp
#include <hip/hip_runtime.h>
#include <hip/hip_bf16.h>
#include <stdint.h>

typedef __attribute__((ext_vector_type(8))) __bf16 bf16x8;
typedef __attribute__((ext_vector_type(4))) float f32x4;

#define STORE_COND
#define FLUSH(p, v) atomicAdd(p, v)
static constexpr int D = 128;
static constexpr int H = 256;
#define NPB_V 20
static constexpr int NPB = NPB_V;
static constexpr int NCHUNK = 128;
static constexpr int CAP = 1024;
static constexpr int MAIN_THREADS = 512;
static constexpr int MAIN_WAVES = MAIN_THREADS / 64;
static constexpr int GRP = 2;
static constexpr int SROW = 260;
static constexpr float NEG_LOG2E = -1.4426950408889634f;
static constexpr float NEG_LN2 = -0.6931471805599453f;

__device__ __forceinline__ float bf16_bits_to_f32(uint32_t b) { return __uint_as_float(b << 16); }

__device__ __forceinline__ uint16_t f32_to_bf16_bits(float f) {
    __hip_bfloat16 h = __float2bfloat16(f);
    return *reinterpret_cast<uint16_t*>(&h);
}

__device__ __forceinline__ uint32_t pack_pair(float lo, float hi) {
    uint32_t l16 = f32_to_bf16_bits(lo);
    uint32_t bits = __float_as_uint(hi);
    uint32_t sign = bits & 0x80000000u;
    uint32_t mag = bits & 0x7fffffffu;
    uint32_t d = mag - l16;
    uint32_t h = min((d + 0x8000u) >> 16, 0x7f7fu);
    h = mag < l16 ? 0u : h;
    h = mag >= 0x7f800000u ? (mag >> 16) : h;
    return sign | (h << 16) | l16;
}

static constexpr int PRE_THREADS = 512;
static constexpr int ST_STRIDE = 132;
static constexpr int WT_STRIDE = 136;

__global__ __launch_bounds__(PRE_THREADS) void prep_kernel(
    const uint16_t* __restrict__ x, const uint16_t* __restrict__ e_attr,
    const int* __restrict__ src, const int* __restrict__ dst,
    const uint16_t* __restrict__ w1, const uint16_t* __restrict__ b1,
    const uint16_t* __restrict__ w2,
    uint16_t* __restrict__ P, float* __restrict__ Q, uint16_t* __restrict__ w2f,
    int2* __restrict__ ent, int2* __restrict__ offs,
    int N, int E, int NB, int chunk_len, int n_gemm_blocks)
{
    __shared__ __attribute__((aligned(16))) unsigned char smem[8 * 16 * ST_STRIDE * 4];
    const int tid = threadIdx.x;
    const int bid = blockIdx.x;

    if (bid < NCHUNK) {
        int* cnt = reinterpret_cast<int*>(smem);
        int* wsum = cnt + 2048;
        int* cntw = cnt + 2048 + 64;
        const int lane = tid & 63, wv = tid >> 6;
        for (int i = tid; i < 8 * NB; i += PRE_THREADS) cntw[i] = 0;
        __syncthreads();
        const int e0 = bid * chunk_len;
        const int e1 = min(E, e0 + chunk_len);
        constexpr int MAXPT = 8;
        int dd[MAXPT], rr[MAXPT];
#pragma unroll
        for (int j = 0; j < MAXPT; ++j) {
            int e = e0 + tid + j * PRE_THREADS;
            dd[j] = dst[min(e, E - 1)];
        }
#pragma unroll
        for (int j = 0; j < MAXPT; ++j) {
            int e = e0 + tid + j * PRE_THREADS;
            rr[j] = 0;
            if (e < e1) rr[j] = atomicAdd(&cntw[wv * NB + dd[j] / NPB], 1);
        }
        int sv[MAXPT];
        uint16_t av[MAXPT];
#pragma unroll
        for (int j = 0; j < MAXPT; ++j) {
            int e = min(e0 + tid + j * PRE_THREADS, E - 1);
            sv[j] = src[e];
            av[j] = e_attr[e];
        }
        __syncthreads();
        for (int i = tid; i < NB; i += PRE_THREADS) {
            int c = 0;
#pragma unroll
            for (int w = 0; w < 8; ++w) { int t = cntw[w * NB + i]; cntw[w * NB + i] = c; c += t; }
            cnt[i] = c;
        }
        __syncthreads();
        const int per = (NB + PRE_THREADS - 1) / PRE_THREADS;
        const int i0 = tid * per;
        int local = 0;
        for (int i = i0; i < min(NB, i0 + per); ++i) local += cnt[i];
        int incl = local;
#pragma unroll
        for (int s = 1; s < 64; s <<= 1) {
            int t = __shfl_up(incl, s, 64);
            if (lane >= s) incl += t;
        }
        if (lane == 63) wsum[wv] = incl;
        __syncthreads();
        int base = 0;
        for (int w = 0; w < wv; ++w) base += wsum[w];
        int run = base + incl - local;
        for (int i = i0; i < min(NB, i0 + per); ++i) { int c = cnt[i]; cnt[i] = run; run += c; }
        __syncthreads();
        if (tid == 0) cnt[NB] = e1 > e0 ? e1 - e0 : 0;
        __syncthreads();
        for (int i = tid; i < NB; i += PRE_THREADS)
            offs[(size_t)i * NCHUNK + bid] = make_int2(e0 + cnt[i], cnt[i + 1] - cnt[i]);
#pragma unroll
        for (int j = 0; j < MAXPT; ++j) {
            int e = e0 + tid + j * PRE_THREADS;
            if (e < e1) {
                int k = dd[j] / NPB;
                int pos = e0 + cnt[k] + cntw[wv * NB + k] + rr[j];
                int2 v;
                v.x = sv[j];
                v.y = ((dd[j] - k * NPB) << 16) | (int)av[j];
                ent[pos] = v;
            }
        }
        return;
    }
    if (bid < NCHUNK + n_gemm_blocks) {
        const int g = bid - NCHUNK;
        const int cb = g & 3;
        const int rb = g >> 2;
        const int krow0 = (cb >> 1) * D;
        const int ccol0 = (cb & 1) * 128;
        const int lane = tid & 63, wv = tid >> 6;
        uint16_t* wt = reinterpret_cast<uint16_t*>(smem);
        bf16x8 bfr[1][4];
        {
            const int nodec = min(rb * 128 + wv * 16 + (lane & 15), N - 1);
#pragma unroll
            for (int ks = 0; ks < 4; ++ks)
                bfr[0][ks] = *reinterpret_cast<const bf16x8*>(x + (size_t)nodec * D + ks * 32 + (lane >> 4) * 8);
        }
        uint2 bv[8];
#pragma unroll
        for (int ct = 0; ct < 8; ++ct)
            bv[ct] = *reinterpret_cast<const uint2*>(b1 + ccol0 + ct * 16 + (lane >> 4) * 4);
        {
            uint32_t v[2][8];
#pragma unroll
            for (int r = 0; r < 2; ++r) {
                const int task = tid + r * PRE_THREADS;
                const int c2 = (task & 63) * 2;
                const int k0 = (task >> 6) * 8;
#pragma unroll
                for (int j = 0; j < 8; ++j)
                    v[r][j] = *reinterpret_cast<const uint32_t*>(w1 + (size_t)(krow0 + k0 + j) * H + ccol0 + c2);
            }
#pragma unroll
            for (int r = 0; r < 2; ++r) {
                const int task = tid + r * PRE_THREADS;
                const int c2 = (task & 63) * 2;
                const int k0 = (task >> 6) * 8;
                uint4 lo, hi;
                lo.x = (v[r][0] & 0xffffu) | (v[r][1] << 16);
                lo.y = (v[r][2] & 0xffffu) | (v[r][3] << 16);
                lo.z = (v[r][4] & 0xffffu) | (v[r][5] << 16);
                lo.w = (v[r][6] & 0xffffu) | (v[r][7] << 16);
                hi.x = (v[r][0] >> 16) | (v[r][1] & 0xffff0000u);
                hi.y = (v[r][2] >> 16) | (v[r][3] & 0xffff0000u);
                hi.z = (v[r][4] >> 16) | (v[r][5] & 0xffff0000u);
                hi.w = (v[r][6] >> 16) | (v[r][7] & 0xffff0000u);
                *reinterpret_cast<uint4*>(wt + c2 * WT_STRIDE + k0) = lo;
                *reinterpret_cast<uint4*>(wt + (c2 + 1) * WT_STRIDE + k0) = hi;
            }
        }
        __syncthreads();
        float* st = reinterpret_cast<float*>(smem) + wv * 16 * ST_STRIDE;
        {
            f32x4 acc[8];
#pragma unroll
            for (int ct = 0; ct < 8; ++ct) acc[ct] = f32x4{0.f, 0.f, 0.f, 0.f};
#pragma unroll
            for (int ks = 0; ks < 4; ++ks) {
#pragma unroll
                for (int ct = 0; ct < 8; ++ct) {
                    bf16x8 afr = *reinterpret_cast<const bf16x8*>(wt + (ct * 16 + (lane & 15)) * WT_STRIDE + ks * 32 + (lane >> 4) * 8);
                    acc[ct] = __builtin_amdgcn_mfma_f32_16x16x32_bf16(afr, bfr[0][ks], acc[ct], 0, 0, 0);
                }
            }
            __syncthreads();
            float* sp = st + (lane & 15) * ST_STRIDE;
            if (cb < 2) {
#pragma unroll
                for (int ct = 0; ct < 8; ++ct) {
                    uint2 v;
                    v.x = pack_pair(acc[ct][0] * NEG_LOG2E, acc[ct][1] * NEG_LOG2E);
                    v.y = pack_pair(acc[ct][2] * NEG_LOG2E, acc[ct][3] * NEG_LOG2E);
                    *reinterpret_cast<uint2*>(sp + ct * 8 + (lane >> 4) * 2) = v;
                }
            } else {
#pragma unroll
                for (int ct = 0; ct < 8; ++ct) {
                    float4 v;
                    v.x = (acc[ct][0] + __uint_as_float(bv[ct].x << 16)) * NEG_LOG2E;
                    v.y = (acc[ct][1] + __uint_as_float(bv[ct].x & 0xffff0000u)) * NEG_LOG2E;
                    v.z = (acc[ct][2] + __uint_as_float(bv[ct].y << 16)) * NEG_LOG2E;
                    v.w = (acc[ct][3] + __uint_as_float(bv[ct].y & 0xffff0000u)) * NEG_LOG2E;
                    *reinterpret_cast<float4*>(sp + ct * 16 + (lane >> 4) * 4) = v;
                }
            }
            __builtin_amdgcn_fence(__ATOMIC_RELEASE, "wavefront");
            __builtin_amdgcn_wave_barrier();
            const int nbase = rb * 128 + wv * 16;
            if (cb < 2) {
#pragma unroll
                for (int r = 0; r < 4; ++r) {
                    const int nl = r * 4 + (lane >> 4);
                    uint4 v = *reinterpret_cast<const uint4*>(st + nl * ST_STRIDE + (lane & 15) * 4);
                    if (nbase + nl < N) *reinterpret_cast<uint4*>(P + (size_t)(nbase + nl) * H + ccol0 + (lane & 15) * 8) = v;
                }
            } else {
#pragma unroll
                for (int r = 0; r < 8; ++r) {
                    const int nl = r * 2 + (lane >> 5);
                    uint4 v = *reinterpret_cast<const uint4*>(st + nl * ST_STRIDE + (lane & 31) * 4);
                    if (nbase + nl < N) *reinterpret_cast<uint4*>(Q + (size_t)(nbase + nl) * H + ccol0 + (lane & 31) * 4) = v;
                }
            }
            __builtin_amdgcn_fence(__ATOMIC_RELEASE, "wavefront");
            __builtin_amdgcn_wave_barrier();
        }
        return;
    }
    const int wb = bid - NCHUNK - n_gemm_blocks;
    if (wb == 0 && tid < H) P[(size_t)N * H + tid] = 0x7F7F;
#pragma unroll
    for (int r = 0; r < 2; ++r) {
        int i = wb * 1024 + r * PRE_THREADS + tid;
        int j = i & 7;
        int lane = (i >> 3) & 63;
        int ks = (i >> 9) & 7;
        int ct = i >> 12;
        int k = ks * 32 + (lane >> 4) * 8 + j;
        int c = ct * 16 + (lane & 15);
        w2f[i] = w2[(size_t)k * D + c];
    }
}

typedef __attribute__((ext_vector_type(2))) float f32x2;

__device__ __forceinline__ void edge4(f32x2& al, f32x2& ah, uint32_t px, uint32_t py, f32x2 ee,
                                      f32x2 wl, f32x2 wh, f32x2 ql, f32x2 qh) {
    f32x2 ul = __builtin_elementwise_fma(ee, wl, ql);
    f32x2 uh = __builtin_elementwise_fma(ee, wh, qh);
    f32x2 pl = {__uint_as_float(px << 16), __uint_as_float(py << 16)};
    f32x2 ph = {__uint_as_float(px), __uint_as_float(py)};
    ul += pl;
    uh += ph;
    f32x2 tl = {__builtin_amdgcn_exp2f(ul.x), __builtin_amdgcn_exp2f(ul.y)};
    f32x2 th = {__builtin_amdgcn_exp2f(uh.x), __builtin_amdgcn_exp2f(uh.y)};
    tl += 1.0f;
    th += 1.0f;
    f32x2 rl = {__builtin_amdgcn_rcpf(tl.x), __builtin_amdgcn_rcpf(tl.y)};
    f32x2 rh = {__builtin_amdgcn_rcpf(th.x), __builtin_amdgcn_rcpf(th.y)};
    al = __builtin_elementwise_fma(ul, rl, al);
    ah = __builtin_elementwise_fma(uh, rh, ah);
}

__global__ __launch_bounds__(MAIN_THREADS, 4) void main_kernel(
    const uint16_t* __restrict__ P, const float* __restrict__ Q,
    const uint16_t* __restrict__ w1, const uint16_t* __restrict__ w2f,
    const uint16_t* __restrict__ b2,
    const int2* __restrict__ ent, const int2* __restrict__ offs,
    uint16_t* __restrict__ out, int N, int NB, int chunk_len)
{
    constexpr int ENT_CAP = CAP + 32 * GRP + 64;
    constexpr int HROW = H + 8;
    static_assert(2 * NPB * HROW * 2 <= NPB * H * 4 + ENT_CAP * 8, "hi/lo tiles must fit over sQ+sEnt");
    __shared__ __attribute__((aligned(16))) float sS[NPB * SROW];
    __shared__ __attribute__((aligned(16))) unsigned char sBuf[NPB * H * 4 + ENT_CAP * 8];
    __shared__ __attribute__((aligned(16))) float sCarry[MAIN_WAVES * H];
    __shared__ int sCarryNode[MAIN_WAVES + 1];
    __shared__ int sCstart[NCHUNK];
    __shared__ int sCpre[NCHUNK + 1];
    __shared__ int sNcnt[MAIN_WAVES * 32];
    __shared__ int sNoff[33];
    __shared__ int sDeg[32];
    float* sQ = reinterpret_cast<float*>(sBuf);
    int2* sEnt = reinterpret_cast<int2*>(sBuf + NPB * H * 4);
    __bf16* sHi = reinterpret_cast<__bf16*>(sBuf);
    __bf16* sLo = sHi + NPB * HROW;

    const int tid = threadIdx.x;
    const int lane = tid & 63;
    const int wv = tid >> 6;
    const int k = blockIdx.x;
    const int n0 = k * NPB;
    const int nn = min(NPB, N - n0);

    if (tid < NCHUNK) {
        const int2 pc = offs[(size_t)k * NCHUNK + tid];
        sCstart[tid] = pc.x;
        sCpre[tid + 1] = pc.y;
    }
    static_assert(NPB * (H / 4) <= 3 * MAIN_THREADS, "Q staging assumes at most 3 float4 per thread");
    const int qn = nn * (H / 4);
    const float4* Qg = reinterpret_cast<const float4*>(Q + (size_t)n0 * H);
    const float4 qr0 = Qg[min(tid, qn - 1)];
    const float4 qr1 = Qg[min(tid + MAIN_THREADS, qn - 1)];
    const float4 qr2 = Qg[min(tid + 2 * MAIN_THREADS, qn - 1)];
    for (int i = tid; i < NPB * SROW; i += MAIN_THREADS) sS[i] = 0.f;
    if (tid < 32) sDeg[tid] = 0;
    if (tid < MAIN_WAVES * 32) sNcnt[tid] = 0;
    if (tid <= MAIN_WAVES) sCarryNode[tid] = -1;
    __syncthreads();
    if (wv == 0) {
        int a = sCpre[2 * lane + 1], b = sCpre[2 * lane + 2];
        int sum = a + b;
        int incl = sum;
#pragma unroll
        for (int d = 1; d < 64; d <<= 1) {
            int t = __shfl_up(incl, d, 64);
            if (lane >= d) incl += t;
        }
        int excl = incl - sum;
        sCpre[2 * lane + 1] = excl + a;
        sCpre[2 * lane + 2] = excl + a + b;
        if (lane == 0) sCpre[0] = 0;
    }
    __syncthreads();
    const int total = sCpre[NCHUNK];

    const int half = lane >> 5;
    const int sl = lane & 31;
    f32x2 wq[4];
    {
        uint4 wv4 = *reinterpret_cast<const uint4*>(w1 + (size_t)(2 * D) * H + sl * 8);
        wq[0] = f32x2{__uint_as_float(wv4.x << 16), __uint_as_float(wv4.y << 16)} * NEG_LOG2E;
        wq[1] = f32x2{__uint_as_float(wv4.x & 0xffff0000u), __uint_as_float(wv4.y & 0xffff0000u)} * NEG_LOG2E;
        wq[2] = f32x2{__uint_as_float(wv4.z << 16), __uint_as_float(wv4.w << 16)} * NEG_LOG2E;
        wq[3] = f32x2{__uint_as_float(wv4.z & 0xffff0000u), __uint_as_float(wv4.w & 0xffff0000u)} * NEG_LOG2E;
    }

    auto stage = [&](int p0, bool first) {
        const int cnt = min(CAP, total - p0);
        int2 ev[CAP / MAIN_THREADS];
        int er[CAP / MAIN_THREADS];
#pragma unroll
        for (int j = 0; j < CAP / MAIN_THREADS; ++j) {
            int p = p0 + min(tid + j * MAIN_THREADS, cnt - 1);
            int lo = 0;
#pragma unroll
            for (int stp = NCHUNK / 2; stp > 0; stp >>= 1) {
                if (sCpre[lo + stp] <= p) lo += stp;
            }
            ev[j] = ent[sCstart[lo] + (p - sCpre[lo])];
        }
        if (first) {
            float4* sQ4 = reinterpret_cast<float4*>(sQ);
            if (tid < qn) sQ4[tid] = qr0;
            if (tid + MAIN_THREADS < qn) sQ4[tid + MAIN_THREADS] = qr1;
            if (tid + 2 * MAIN_THREADS < qn) sQ4[tid + 2 * MAIN_THREADS] = qr2;
        }
#pragma unroll
        for (int j = 0; j < CAP / MAIN_THREADS; ++j) {
            er[j] = 0;
            if (tid + j * MAIN_THREADS < cnt) er[j] = atomicAdd(&sNcnt[wv * 32 + (ev[j].y >> 16)], 1);
        }
        __syncthreads();
        if (wv == 0) {
            int c = 0;
            if (lane < 32) {
#pragma unroll
                for (int w = 0; w < MAIN_WAVES; ++w) {
                    int t = sNcnt[w * 32 + lane];
                    sNcnt[w * 32 + lane] = c;
                    c += t;
                }
            }
            int cp = (c + GRP - 1) / GRP * GRP;
            int incl = cp;
#pragma unroll
            for (int d = 1; d < 32; d <<= 1) {
                int t = __shfl_up(incl, d, 64);
                if (lane >= d) incl += t;
            }
            if (lane < 32) {
                int st = incl - cp;
                sNoff[lane] = st;
                sDeg[lane] += c;
                if (lane == 31) sNoff[32] = incl;
                int2 pad; pad.x = N * (H * 2); pad.y = lane << 16;
                for (int i = c; i < cp; ++i) sEnt[st + i] = pad;
            }
        }
        __syncthreads();
#pragma unroll
        for (int j = 0; j < CAP / MAIN_THREADS; ++j) {
            int i = tid + j * MAIN_THREADS;
            const int nd = ev[j].y >> 16;
            if (i < cnt) sEnt[sNoff[nd] + sNcnt[wv * 32 + nd] + er[j]] = make_int2(ev[j].x * (H * 2), ev[j].y);
        }
        __syncthreads();
    };

    auto merge = [&]() {
        const int nd = sCarryNode[wv];
        if (nd >= 0 && (wv == 0 || sCarryNode[wv - 1] != nd)) {
            float4 v = *reinterpret_cast<const float4*>(sS + nd * SROW + lane * 4);
            for (int r = wv; r < MAIN_WAVES && sCarryNode[r] == nd; ++r) {
                float4 cvv = *reinterpret_cast<const float4*>(sCarry + r * H + lane * 4);
                v.x += cvv.x; v.y += cvv.y; v.z += cvv.z; v.w += cvv.w;
            }
            *reinterpret_cast<float4*>(sS + nd * SROW + lane * 4) = v;
        }
    };

    if (total > 0) stage(0, true);
    for (int p0 = 0; p0 < total; p0 += CAP) {
        const int nslot = sNoff[32] / 2;
        const int sper = (nslot + MAIN_WAVES - 1) / MAIN_WAVES;
        const int s0 = min(nslot, wv * sper);
        const int c0 = __builtin_amdgcn_readfirstlane(s0 * 2);
        const int n = __builtin_amdgcn_readfirstlane((min(nslot, s0 + sper) - s0) * 2);
        if (n > 0) {
            int cur = -1;
            int nseg = 0;
            f32x2 q[4], acc[4];
#pragma unroll
            for (int j = 0; j < 4; ++j) { q[j] = f32x2{0.f, 0.f}; acc[j] = f32x2{0.f, 0.f}; }
            auto flush = [&]() {
                float a8[8] = {acc[0].x, acc[1].x, acc[0].y, acc[1].y, acc[2].x, acc[3].x, acc[2].y, acc[3].y};
                float v[8];
#pragma unroll
                for (int j = 0; j < 8; ++j) v[j] = a8[j] + __shfl_xor(a8[j], 32, 64);
#pragma unroll
                for (int j = 0; j < 4; ++j) acc[j] = f32x2{0.f, 0.f};
                if (half == 0) {
                    float* dp;
                    if (nseg > 0) {
                        dp = sS + cur * SROW + sl * 8;
                        float4 o0 = *reinterpret_cast<const float4*>(dp);
                        float4 o1 = *reinterpret_cast<const float4*>(dp + 4);
                        v[0] += o0.x; v[1] += o0.y; v[2] += o0.z; v[3] += o0.w;
                        v[4] += o1.x; v[5] += o1.y; v[6] += o1.z; v[7] += o1.w;
                    } else {
                        dp = sCarry + wv * H + sl * 8;
                    }
                    *reinterpret_cast<float4*>(dp) = make_float4(v[0], v[1], v[2], v[3]);
                    *reinterpret_cast<float4*>(dp + 4) = make_float4(v[4], v[5], v[6], v[7]);
                }
                if (nseg == 0 && lane == 0) sCarryNode[wv] = cur;
                ++nseg;
            };
            constexpr int NS = 4;
            uint4 pvA[NS], pvB[NS];
            int yyA[NS], yyB[NS];
            const unsigned char* Pb = reinterpret_cast<const unsigned char*>(P);
            const uint32_t loff = sl * 16;
            const int2* sE = sEnt + c0 + half;
            const int last = n - 2;
            int2 en[NS];
            auto read_entries = [&](int i) {
#pragma unroll
                for (int u = 0; u < NS; ++u) en[u] = sE[min(i + 2 * u, last)];
            };
            auto issue_loads = [&](uint4* pv, int* yy) {
#pragma unroll
                for (int u = 0; u < NS; ++u) {
                    yy[u] = en[u].y;
                    pv[u] = *reinterpret_cast<const uint4*>(Pb + ((uint32_t)en[u].x + loff));
                }
            };
            auto compute_slot = [&](const uint4& pv, int yy) {
                int node = __builtin_amdgcn_readfirstlane(yy) >> 16;
                if (node != cur) {
                    if (cur >= 0) flush();
                    float4 qa = *reinterpret_cast<const float4*>(sQ + node * H + sl * 8);
                    float4 qb = *reinterpret_cast<const float4*>(sQ + node * H + sl * 8 + 4);
                    q[0] = f32x2{qa.x, qa.z}; q[1] = f32x2{qa.y, qa.w};
                    q[2] = f32x2{qb.x, qb.z}; q[3] = f32x2{qb.y, qb.w};
                    cur = node;
                }
                float e = __uint_as_float((uint32_t)yy << 16);
                f32x2 ee = {e, e};
                edge4(acc[0], acc[1], pv.x, pv.y, ee, wq[0], wq[1], q[0], q[1]);
                edge4(acc[2], acc[3], pv.z, pv.w, ee, wq[2], wq[3], q[2], q[3]);
            };
            read_entries(0);
            issue_loads(pvA, yyA);
            read_entries(2 * NS);
            for (int i = 0; i < n; i += 4 * NS) {
                issue_loads(pvB, yyB);
                read_entries(i + 4 * NS);
#pragma unroll
                for (int u = 0; u < NS; ++u)
                    if (i + 2 * u < n) compute_slot(pvA[u], yyA[u]);
                issue_loads(pvA, yyA);
                read_entries(i + 6 * NS);
#pragma unroll
                for (int u = 0; u < NS; ++u)
                    if (i + 2 * NS + 2 * u < n) compute_slot(pvB[u], yyB[u]);
            }
            flush();
        }
        if (p0 + CAP >= total) break;
        __syncthreads();
        merge();
        __syncthreads();
        if (tid < MAIN_WAVES * 32) sNcnt[tid] = 0;
        if (tid <= MAIN_WAVES) sCarryNode[tid] = -1;
        __syncthreads();
        stage(p0 + CAP, false);
    }

    bf16x8 afr[8];
#pragma unroll
    for (int ks = 0; ks < 8; ++ks)
        afr[ks] = *reinterpret_cast<const bf16x8*>(w2f + ((size_t)(wv * 8 + ks) * 64 + lane) * 8);
    const uint2 bbv = *reinterpret_cast<const uint2*>(b2 + wv * 16 + (lane >> 4) * 4);
    __syncthreads();
    merge();
    __syncthreads();
    for (int i = tid; i < NPB * (H / 2); i += MAIN_THREADS) {
        const int row = i / (H / 2), c2 = (i % (H / 2)) * 2;
        const float2 sv = *reinterpret_cast<const float2*>(sS + row * SROW + c2);
        const float s0 = sv.x * NEG_LN2, s1 = sv.y * NEG_LN2;
        const __bf16 h0 = (__bf16)s0, h1 = (__bf16)s1;
        const __bf16 l0 = (__bf16)(s0 - (float)h0), l1 = (__bf16)(s1 - (float)h1);
        typedef __attribute__((ext_vector_type(2))) __bf16 bf16x2;
        *reinterpret_cast<bf16x2*>(sHi + row * HROW + c2) = bf16x2{h0, h1};
        *reinterpret_cast<bf16x2*>(sLo + row * HROW + c2) = bf16x2{l0, l1};
    }
    __syncthreads();

    {
        const int cc = wv * 16 + (lane >> 4) * 4;
        float bb[4] = {__uint_as_float(bbv.x << 16), __uint_as_float(bbv.x & 0xffff0000u),
                       __uint_as_float(bbv.y << 16), __uint_as_float(bbv.y & 0xffff0000u)};
#pragma unroll
        for (int nt = 0; nt < 2; ++nt) {
            const int nl = nt * 16 + (lane & 15);
            const int row = min(nl, NPB - 1);
            f32x4 acc = {0.f, 0.f, 0.f, 0.f};
#pragma unroll
            for (int ks = 0; ks < 8; ++ks) {
                bf16x8 hi = *reinterpret_cast<const bf16x8*>(sHi + row * HROW + ks * 32 + (lane >> 4) * 8);
                bf16x8 lo = *reinterpret_cast<const bf16x8*>(sLo + row * HROW + ks * 32 + (lane >> 4) * 8);
                acc = __builtin_amdgcn_mfma_f32_16x16x32_bf16(afr[ks], hi, acc, 0, 0, 0);
                acc = __builtin_amdgcn_mfma_f32_16x16x32_bf16(afr[ks], lo, acc, 0, 0, 0);
            }
            if (nl < nn) {
                float dg = (float)sDeg[nl];
                uint32_t o0 = (uint32_t)f32_to_bf16_bits(acc[0] + dg * bb[0]) | ((uint32_t)f32_to_bf16_bits(acc[1] + dg * bb[1]) << 16);
                uint32_t o1 = (uint32_t)f32_to_bf16_bits(acc[2] + dg * bb[2]) | ((uint32_t)f32_to_bf16_bits(acc[3] + dg * bb[3]) << 16);
                uint2 v; v.x = o0; v.y = o1;
                *reinterpret_cast<uint2*>(out + (size_t)(n0 + nl) * D + cc) = v;
            }
        }
    }
}

static inline size_t align_up(size_t v, size_t a) { return (v + a - 1) / a * a; }

extern "C" void kernel_launch(void* const* d_in, const int* in_sizes, int n_in,
                              void* d_out, int out_size, void* d_ws, size_t ws_size,
                              hipStream_t stream) {
    const uint16_t* x = (const uint16_t*)d_in[0];
    const uint16_t* e_attr = (const uint16_t*)d_in[1];
    const int* src = (const int*)d_in[2];
    const int* dst = (const int*)d_in[3];
    const uint16_t* w1 = (const uint16_t*)d_in[4];
    const uint16_t* b1 = (const uint16_t*)d_in[5];
    const uint16_t* w2 = (const uint16_t*)d_in[6];
    const uint16_t* b2 = (const uint16_t*)d_in[7];
    uint16_t* out = (uint16_t*)d_out;

    const int N = in_sizes[0] / D;
    const int E = in_sizes[2];
    const int NB = (N + NPB - 1) / NPB;
    const int chunk_len = (E + NCHUNK - 1) / NCHUNK;
    const int n_gemm_blocks = ((N + 127) / 128) * 4;

    unsigned char* ws = (unsigned char*)d_ws;
    size_t off = 0;
    uint16_t* P = (uint16_t*)(ws + off); off = align_up(off + (size_t)(N + 1) * H * 2, 256);
    float* Q = (float*)(ws + off); off = align_up(off + (size_t)N * H * 4, 256);
    uint16_t* w2f = (uint16_t*)(ws + off); off = align_up(off + (size_t)H * D * 2, 256);
    int2* ent = (int2*)(ws + off); off = align_up(off + (size_t)NCHUNK * chunk_len * 8, 256);
    int2* offs = (int2*)(ws + off); off = align_up(off + (size_t)NCHUNK * NB * 8, 256);

    prep_kernel<<<NCHUNK + n_gemm_blocks + 32, PRE_THREADS, 0, stream>>>(
        x, e_attr, src, dst, w1, b1, w2, P, Q, w2f, ent, offs, N, E, NB, chunk_len, n_gemm_blocks);
    main_kernel<<<NB, MAIN_THREADS, 0, stream>>>(P, Q, w1, w2f, b2, ent, offs, out, N, NB, chunk_len);
}
```

```cpp
#include <hip/hip_runtime.h>
#include <hip/hip_bf16.h>
#include <stdint.h>

typedef __attribute__((ext_vector_type(8))) __bf16 bf16x8;
typedef __attribute__((ext_vector_type(4))) float f32x4;

static constexpr int D = 128;
static constexpr int H = 256;
static constexpr int NPB = 20;
static constexpr int NCHUNK = 128;
static constexpr int CAP = 1024;
static constexpr int MAIN_THREADS = 512;
static constexpr int MAIN_WAVES = MAIN_THREADS / 64;
static constexpr int GRP = 2;
static constexpr int SROW = 260;
static constexpr float NEG_LOG2E = -1.4426950408889634f;
static constexpr float NEG_LN2 = -0.6931471805599453f;

__device__ __forceinline__ float bf16_bits_to_f32(uint32_t b) { return __uint_as_float(b << 16); }

__device__ __forceinline__ uint16_t f32_to_bf16_bits(float f) {
    __hip_bfloat16 h = __float2bfloat16(f);
    return *reinterpret_cast<uint16_t*>(&h);
}

__device__ __forceinline__ uint32_t pack_pair(float lo, float hi) {
    uint32_t l16 = f32_to_bf16_bits(lo);
    uint32_t bits = __float_as_uint(hi);
    uint32_t sign = bits & 0x80000000u;
    uint32_t mag = bits & 0x7fffffffu;
    uint32_t d = mag - l16;
    uint32_t h = min((d + 0x8000u) >> 16, 0x7f7fu);
    h = mag < l16 ? 0u : h;
    h = mag >= 0x7f800000u ? (mag >> 16) : h;
    return sign | (h << 16) | l16;
}

static constexpr int PRE_THREADS = 512;
static constexpr int ST_STRIDE = 132;
static constexpr int WT_STRIDE = 136;

__global__ __launch_bounds__(PRE_THREADS) void prep_kernel(
    const uint16_t* __restrict__ x, const uint16_t* __restrict__ e_attr,
    const int* __restrict__ src, const int* __restrict__ dst,
    const uint16_t* __restrict__ w1, const uint16_t* __restrict__ b1,
    const uint16_t* __restrict__ w2,
    uint16_t* __restrict__ P, float* __restrict__ Q, uint16_t* __restrict__ w2f,
    int2* __restrict__ ent, int2* __restrict__ offs,
    int N, int E, int NB, int chunk_len, int n_gemm_blocks)
{
    __shared__ __attribute__((aligned(16))) unsigned char smem[8 * 16 * ST_STRIDE * 4];
    const int tid = threadIdx.x;
    const int bid = blockIdx.x;

    if (bid < NCHUNK) {
        int* cnt = reinterpret_cast<int*>(smem);
        int* wsum = cnt + 2048;
        int* cntw = cnt + 2048 + 64;
        const int lane = tid & 63, wv = tid >> 6;
        for (int i = tid; i < 8 * NB; i += PRE_THREADS) cntw[i] = 0;
        __syncthreads();
        const int e0 = bid * chunk_len;
        const int e1 = min(E, e0 + chunk_len);
        constexpr int MAXPT = 8;
        int dd[MAXPT], kk[MAXPT], rr[MAXPT];
#pragma unroll
        for (int j = 0; j < MAXPT; ++j) {
            int e = e0 + tid + j * PRE_THREADS;
            dd[j] = dst[min(e, E - 1)];
        }
#pragma unroll
        for (int j = 0; j < MAXPT; ++j) kk[j] = (int)(((unsigned)dd[j] * (unsigned)NB) / (unsigned)N);
#pragma unroll
        for (int j = 0; j < MAXPT; ++j) {
            int e = e0 + tid + j * PRE_THREADS;
            rr[j] = 0;
            if (e < e1) rr[j] = atomicAdd(&cntw[wv * NB + kk[j]], 1);
        }
        int sv[MAXPT];
        uint16_t av[MAXPT];
#pragma unroll
        for (int j = 0; j < MAXPT; ++j) {
            int e = min(e0 + tid + j * PRE_THREADS, E - 1);
            sv[j] = src[e];
            av[j] = e_attr[e];
        }
        __syncthreads();
        for (int i = tid; i < NB; i += PRE_THREADS) {
            int c = 0;
#pragma unroll
            for (int w = 0; w < 8; ++w) { int t = cntw[w * NB + i]; cntw[w * NB + i] = c; c += t; }
            cnt[i] = c;
        }
        __syncthreads();
        const int per = (NB + PRE_THREADS - 1) / PRE_THREADS;
        const int i0 = tid * per;
        int local = 0;
        for (int i = i0; i < min(NB, i0 + per); ++i) local += cnt[i];
        int incl = local;
#pragma unroll
        for (int s = 1; s < 64; s <<= 1) {
            int t = __shfl_up(incl, s, 64);
            if (lane >= s) incl += t;
        }
        if (lane == 63) wsum[wv] = incl;
        __syncthreads();
        int base = 0;
        for (int w = 0; w < wv; ++w) base += wsum[w];
        int run = base + incl - local;
        for (int i = i0; i < min(NB, i0 + per); ++i) { int c = cnt[i]; cnt[i] = run; run += c; }
        __syncthreads();
        if (tid == 0) cnt[NB] = e1 > e0 ? e1 - e0 : 0;
        __syncthreads();
        for (int i = tid; i < NB; i += PRE_THREADS)
            offs[(size_t)i * NCHUNK + bid] = make_int2(e0 + cnt[i], cnt[i + 1] - cnt[i]);
#pragma unroll
        for (int j = 0; j < MAXPT; ++j) {
            int e = e0 + tid + j * PRE_THREADS;
            if (e < e1) {
                int k = kk[j];
                int pos = e0 + cnt[k] + cntw[wv * NB + k] + rr[j];
                int nstart = (int)(((unsigned)k * (unsigned)N + (unsigned)NB - 1u) / (unsigned)NB);
                int2 v;
                v.x = sv[j];
                v.y = ((dd[j] - nstart) << 16) | (int)av[j];
                ent[pos] = v;
            }
        }
        return;
    }
    if (bid < NCHUNK + n_gemm_blocks) {
        const int g = bid - NCHUNK;
        const int cb = g & 3;
        const int rb = g >> 2;
        const int krow0 = (cb >> 1) * D;
        const int ccol0 = (cb & 1) * 128;
        const int lane = tid & 63, wv = tid >> 6;
        uint16_t* wt = reinterpret_cast<uint16_t*>(smem);
        bf16x8 bfr[1][4];
        {
            const int nodec = min(rb * 128 + wv * 16 + (lane & 15), N - 1);
#pragma unroll
            for (int ks = 0; ks < 4; ++ks)
                bfr[0][ks] = *reinterpret_cast<const bf16x8*>(x + (size_t)nodec * D + ks * 32 + (lane >> 4) * 8);
        }
        uint2 bv[8];
#pragma unroll
        for (int ct = 0; ct < 8; ++ct)
            bv[ct] = *reinterpret_cast<const uint2*>(b1 + ccol0 + ct * 16 + (lane >> 4) * 4);
        {
            uint32_t v[2][8];
#pragma unroll
            for (int r = 0; r < 2; ++r) {
                const int task = tid + r * PRE_THREADS;
                const int c2 = (task & 63) * 2;
                const int k0 = (task >> 6) * 8;
#pragma unroll
                for (int j = 0; j < 8; ++j)
                    v[r][j] = *reinterpret_cast<const uint32_t*>(w1 + (size_t)(krow0 + k0 + j) * H + ccol0 + c2);
            }
#pragma unroll
            for (int r = 0; r < 2; ++r) {
                const int task = tid + r * PRE_THREADS;
                const int c2 = (task & 63) * 2;
                const int k0 = (task >> 6) * 8;
                uint4 lo, hi;
                lo.x = (v[r][0] & 0xffffu) | (v[r][1] << 16);
                lo.y = (v[r][2] & 0xffffu) | (v[r][3] << 16);
                lo.z = (v[r][4] & 0xffffu) | (v[r][5] << 16);
                lo.w = (v[r][6] & 0xffffu) | (v[r][7] << 16);
                hi.x = (v[r][0] >> 16) | (v[r][1] & 0xffff0000u);
                hi.y = (v[r][2] >> 16) | (v[r][3] & 0xffff0000u);
                hi.z = (v[r][4] >> 16) | (v[r][5] & 0xffff0000u);
                hi.w = (v[r][6] >> 16) | (v[r][7] & 0xffff0000u);
                *reinterpret_cast<uint4*>(wt + c2 * WT_STRIDE + k0) = lo;
                *reinterpret_cast<uint4*>(wt + (c2 + 1) * WT_STRIDE + k0) = hi;
            }
        }
        __syncthreads();
        float* st = reinterpret_cast<float*>(smem) + wv * 16 * ST_STRIDE;
        {
            f32x4 acc[8];
#pragma unroll
            for (int ct = 0; ct < 8; ++ct) acc[ct] = f32x4{0.f, 0.f, 0.f, 0.f};
#pragma unroll
            for (int ks = 0; ks < 4; ++ks) {
#pragma unroll
                for (int ct = 0; ct < 8; ++ct) {
                    bf16x8 afr = *reinterpret_cast<const bf16x8*>(wt + (ct * 16 + (lane & 15)) * WT_STRIDE + ks * 32 + (lane >> 4) * 8);
                    acc[ct] = __builtin_amdgcn_mfma_f32_16x16x32_bf16(afr, bfr[0][ks], acc[ct], 0, 0, 0);
                }
            }
            __syncthreads();
            float* sp = st + (lane & 15) * ST_STRIDE;
            if (cb < 2) {
#pragma unroll
                for (int ct = 0; ct < 8; ++ct) {
                    uint2 v;
                    v.x = pack_pair(acc[ct][0] * NEG_LOG2E, acc[ct][1] * NEG_LOG2E);
                    v.y = pack_pair(acc[ct][2] * NEG_LOG2E, acc[ct][3] * NEG_LOG2E);
                    *reinterpret_cast<uint2*>(sp + ct * 8 + (lane >> 4) * 2) = v;
                }
            } else {
#pragma unroll
                for (int ct = 0; ct < 8; ++ct) {
                    float4 v;
                    v.x = (acc[ct][0] + __uint_as_float(bv[ct].x << 16)) * NEG_LOG2E;
                    v.y = (acc[ct][1] + __uint_as_float(bv[ct].x & 0xffff0000u)) * NEG_LOG2E;
                    v.z = (acc[ct][2] + __uint_as_float(bv[ct].y << 16)) * NEG_LOG2E;
                    v.w = (acc[ct][3] + __uint_as_float(bv[ct].y & 0xffff0000u)) * NEG_LOG2E;
                    *reinterpret_cast<float4*>(sp + ct * 16 + (lane >> 4) * 4) = v;
                }
            }
            __builtin_amdgcn_fence(__ATOMIC_RELEASE, "wavefront");
            __builtin_amdgcn_wave_barrier();
            const int nbase = rb * 128 + wv * 16;
            if (cb < 2) {
#pragma unroll
                for (int r = 0; r < 4; ++r) {
                    const int nl = r * 4 + (lane >> 4);
                    uint4 v = *reinterpret_cast<const uint4*>(st + nl * ST_STRIDE + (lane & 15) * 4);
                    if (nbase + nl < N) *reinterpret_cast<uint4*>(P + (size_t)(nbase + nl) * H + ccol0 + (lane & 15) * 8) = v;
                }
            } else {
#pragma unroll
                for (int r = 0; r < 8; ++r) {
                    const int nl = r * 2 + (lane >> 5);
                    uint4 v = *reinterpret_cast<const uint4*>(st + nl * ST_STRIDE + (lane & 31) * 4);
                    if (nbase + nl < N) *reinterpret_cast<uint4*>(Q + (size_t)(nbase + nl) * H + ccol0 + (lane & 31) * 4) = v;
                }
            }
            __builtin_amdgcn_fence(__ATOMIC_RELEASE, "wavefront");
            __builtin_amdgcn_wave_barrier();
        }
        return;
    }
    const int wb = bid - NCHUNK - n_gemm_blocks;
    if (wb == 0 && tid < H) P[(size_t)N * H + tid] = 0x7F7F;
#pragma unroll
    for (int r = 0; r < 2; ++r) {
        int i = wb * 1024 + r * PRE_THREADS + tid;
        int j = i & 7;
        int lane = (i >> 3) & 63;
        int ks = (i >> 9) & 7;
        int ct = i >> 12;
        int k = ks * 32 + (lane >> 4) * 8 + j;
        int c = ct * 16 + (lane & 15);
        w2f[i] = w2[(size_t)k * D + c];
    }
}

typedef __attribute__((ext_vector_type(2))) float f32x2;

__device__ __forceinline__ void edge4(f32x2& al, f32x2& ah, uint32_t px, uint32_t py, f32x2 ee,
                                      f32x2 wl, f32x2 wh, f32x2 ql, f32x2 qh) {
    f32x2 ul = __builtin_elementwise_fma(ee, wl, ql);
    f32x2 uh = __builtin_elementwise_fma(ee, wh, qh);
    f32x2 pl = {__uint_as_float(px << 16), __uint_as_float(py << 16)};
    f32x2 ph = {__uint_as_float(px), __uint_as_float(py)};
    ul += pl;
    uh += ph;
    f32x2 tl = {__builtin_amdgcn_exp2f(ul.x), __builtin_amdgcn_exp2f(ul.y)};
    f32x2 th = {__builtin_amdgcn_exp2f(uh.x), __builtin_amdgcn_exp2f(uh.y)};
    tl += 1.0f;
    th += 1.0f;
    f32x2 rl = {__builtin_amdgcn_rcpf(tl.x), __builtin_amdgcn_rcpf(tl.y)};
    f32x2 rh = {__builtin_amdgcn_rcpf(th.x), __builtin_amdgcn_rcpf(th.y)};
    al = __builtin_elementwise_fma(ul, rl, al);
    ah = __builtin_elementwise_fma(uh, rh, ah);
}

__global__ __launch_bounds__(MAIN_THREADS, 4) void main_kernel(
    const uint16_t* __restrict__ P, const float* __restrict__ Q,
    const uint16_t* __restrict__ w1, const uint16_t* __restrict__ w2f,
    const uint16_t* __restrict__ b2,
    const int2* __restrict__ ent, const int2* __restrict__ offs,
    uint16_t* __restrict__ out, int N, int NB, int chunk_len)
{
    constexpr int ENT_CAP = CAP + 32 * GRP + 64;
    constexpr int HROW = H + 8;
    static_assert(2 * NPB * HROW * 2 <= NPB * H * 4 + ENT_CAP * 8, "hi/lo tiles must fit over sQ+sEnt");
    __shared__ __attribute__((aligned(16))) float sS[NPB * SROW];
    __shared__ __attribute__((aligned(16))) unsigned char sBuf[NPB * H * 4 + ENT_CAP * 8];
    __shared__ __attribute__((aligned(16))) float sCarry[MAIN_WAVES * H];
    __shared__ int sCarryNode[MAIN_WAVES + 1];
    __shared__ int sCstart[NCHUNK];
    __shared__ int sCpre[NCHUNK + 1];
    __shared__ int sNcnt[MAIN_WAVES * 32];
    __shared__ int sNoff[33];
    __shared__ int sDeg[32];
    float* sQ = reinterpret_cast<float*>(sBuf);
    int2* sEnt = reinterpret_cast<int2*>(sBuf + NPB * H * 4);
    __bf16* sHi = reinterpret_cast<__bf16*>(sBuf);
    __bf16* sLo = sHi + NPB * HROW;

    const int tid = threadIdx.x;
    const int lane = tid & 63;
    const int wv = tid >> 6;
    const int k = blockIdx.x;
    const int n0 = (int)(((unsigned)k * (unsigned)N + (unsigned)NB - 1u) / (unsigned)NB);
    const int nn = (int)(((unsigned)(k + 1) * (unsigned)N + (unsigned)NB - 1u) / (unsigned)NB) - n0;

    if (tid < NCHUNK) {
        const int2 pc = offs[(size_t)k * NCHUNK + tid];
        sCstart[tid] = pc.x;
        sCpre[tid + 1] = pc.y;
    }
    static_assert(NPB * (H / 4) <= 3 * MAIN_THREADS, "Q staging assumes at most 3 float4 per thread");
    const int qn = nn * (H / 4);
    const float4* Qg = reinterpret_cast<const float4*>(Q + (size_t)n0 * H);
    const int qlast = max(qn - 1, 0);
    const float4 qr0 = Qg[min(tid, qlast)];
    const float4 qr1 = Qg[min(tid + MAIN_THREADS, qlast)];
    const float4 qr2 = Qg[min(tid + 2 * MAIN_THREADS, qlast)];
    for (int i = tid; i < NPB * SROW; i += MAIN_THREADS) sS[i] = 0.f;
    if (tid < 32) sDeg[tid] = 0;
    if (tid < MAIN_WAVES * 32) sNcnt[tid] = 0;
    if (tid <= MAIN_WAVES) sCarryNode[tid] = -1;
    __syncthreads();
    if (wv == 0) {
        int a = sCpre[2 * lane + 1], b = sCpre[2 * lane + 2];
        int sum = a + b;
        int incl = sum;
#pragma unroll
        for (int d = 1; d < 64; d <<= 1) {
            int t = __shfl_up(incl, d, 64);
            if (lane >= d) incl += t;
        }
        int excl = incl - sum;
        sCpre[2 * lane + 1] = excl + a;
        sCpre[2 * lane + 2] = excl + a + b;
        if (lane == 0) sCpre[0] = 0;
    }
    __syncthreads();
    const int total = sCpre[NCHUNK];

    const int half = lane >> 5;
    const int sl = lane & 31;
    f32x2 wq[4];
    {
        uint4 wv4 = *reinterpret_cast<const uint4*>(w1 + (size_t)(2 * D) * H + sl * 8);
        wq[0] = f32x2{__uint_as_float(wv4.x << 16), __uint_as_float(wv4.y << 16)} * NEG_LOG2E;
        wq[1] = f32x2{__uint_as_float(wv4.x & 0xffff0000u), __uint_as_float(wv4.y & 0xffff0000u)} * NEG_LOG2E;
        wq[2] = f32x2{__uint_as_float(wv4.z << 16), __uint_as_float(wv4.w << 16)} * NEG_LOG2E;
        wq[3] = f32x2{__uint_as_float(wv4.z & 0xffff0000u), __uint_as_float(wv4.w & 0xffff0000u)} * NEG_LOG2E;
    }

    auto stage = [&](int p0, bool first) {
        const int cnt = min(CAP, total - p0);
        int2 ev[CAP / MAIN_THREADS];
        int er[CAP / MAIN_THREADS];
#pragma unroll
        for (int j = 0; j < CAP / MAIN_THREADS; ++j) {
            int p = p0 + min(tid + j * MAIN_THREADS, cnt - 1);
            int lo = 0;
#pragma unroll
            for (int stp = NCHUNK / 2; stp > 0; stp >>= 1) {
                if (sCpre[lo + stp] <= p) lo += stp;
            }
            ev[j] = ent[sCstart[lo] + (p - sCpre[lo])];
        }
        if (first) {
            float4* sQ4 = reinterpret_cast<float4*>(sQ);
            if (tid < qn) sQ4[tid] = qr0;
            if (tid + MAIN_THREADS < qn) sQ4[tid + MAIN_THREADS] = qr1;
            if (tid + 2 * MAIN_THREADS < qn) sQ4[tid + 2 * MAIN_THREADS] = qr2;
        }
#pragma unroll
        for (int j = 0; j < CAP / MAIN_THREADS; ++j) {
            er[j] = 0;
            if (tid + j * MAIN_THREADS < cnt) er[j] = atomicAdd(&sNcnt[wv * 32 + (ev[j].y >> 16)], 1);
        }
        __syncthreads();
        if (wv == 0) {
            int c = 0;
            if (lane < 32) {
#pragma unroll
                for (int w = 0; w < MAIN_WAVES; ++w) {
                    int t = sNcnt[w * 32 + lane];
                    sNcnt[w * 32 + lane] = c;
                    c += t;
                }
            }
            int cp = (c + GRP - 1) / GRP * GRP;
            int incl = cp;
#pragma unroll
            for (int d = 1; d < 32; d <<= 1) {
                int t = __shfl_up(incl, d, 64);
                if (lane >= d) incl += t;
            }
            if (lane < 32) {
                int st = incl - cp;
                sNoff[lane] = st;
                sDeg[lane] += c;
                if (lane == 31) sNoff[32] = incl;
                int2 pad; pad.x = N * (H * 2); pad.y = lane << 16;
                for (int i = c; i < cp; ++i) sEnt[st + i] = pad;
            }
        }
        __syncthreads();
#pragma unroll
        for (int j = 0; j < CAP / MAIN_THREADS; ++j) {
            int i = tid + j * MAIN_THREADS;
            const int nd = ev[j].y >> 16;
            if (i < cnt) sEnt[sNoff[nd] + sNcnt[wv * 32 + nd] + er[j]] = make_int2(ev[j].x * (H * 2), ev[j].y);
        }
        __syncthreads();
    };

    auto merge = [&]() {
        const int nd = sCarryNode[wv];
        if (nd >= 0 && (wv == 0 || sCarryNode[wv - 1] != nd)) {
            float4 v = *reinterpret_cast<const float4*>(sS + nd * SROW + lane * 4);
            for (int r = wv; r < MAIN_WAVES && sCarryNode[r] == nd; ++r) {
                float4 cvv = *reinterpret_cast<const float4*>(sCarry + r * H + lane * 4);
                v.x += cvv.x; v.y += cvv.y; v.z += cvv.z; v.w += cvv.w;
            }
            *reinterpret_cast<float4*>(sS + nd * SROW + lane * 4) = v;
        }
    };

    if (total > 0) stage(0, true);
    for (int p0 = 0; p0 < total; p0 += CAP) {
        const int nslot = sNoff[32] / 2;
        const int sper = (nslot + MAIN_WAVES - 1) / MAIN_WAVES;
        const int s0 = min(nslot, wv * sper);
        const int c0 = __builtin_amdgcn_readfirstlane(s0 * 2);
        const int n = __builtin_amdgcn_readfirstlane((min(nslot, s0 + sper) - s0) * 2);
        if (n > 0) {
            int cur = -1;
            int nseg = 0;
            f32x2 q[4], acc[4];
#pragma unroll
            for (int j = 0; j < 4; ++j) { q[j] = f32x2{0.f, 0.f}; acc[j] = f32x2{0.f, 0.f}; }
            auto flush = [&]() {
                float a8[8] = {acc[0].x, acc[1].x, acc[0].y, acc[1].y, acc[2].x, acc[3].x, acc[2].y, acc[3].y};
                float v[8];
#pragma unroll
                for (int j = 0; j < 8; ++j) v[j] = a8[j] + __shfl_xor(a8[j], 32, 64);
#pragma unroll
                for (int j = 0; j < 4; ++j) acc[j] = f32x2{0.f, 0.f};
                if (half == 0) {
                    float* dp;
                    if (nseg > 0) {
                        dp = sS + cur * SROW + sl * 8;
                        float4 o0 = *reinterpret_cast<const float4*>(dp);
                        float4 o1 = *reinterpret_cast<const float4*>(dp + 4);
                        v[0] += o0.x; v[1] += o0.y; v[2] += o0.z; v[3] += o0.w;
                        v[4] += o1.x; v[5] += o1.y; v[6] += o1.z; v[7] += o1.w;
                    } else {
                        dp = sCarry + wv * H + sl * 8;
                    }
                    *reinterpret_cast<float4*>(dp) = make_float4(v[0], v[1], v[2], v[3]);
                    *reinterpret_cast<float4*>(dp + 4) = make_float4(v[4], v[5], v[6], v[7]);
                }
                if (nseg == 0 && lane == 0) sCarryNode[wv] = cur;
                ++nseg;
            };
            constexpr int NS = 4;
            uint4 pvA[NS], pvB[NS];
            int yyA[NS], yyB[NS];
            const unsigned char* Pb = reinterpret_cast<const unsigned char*>(P);
            const uint32_t loff = sl * 16;
            const int2* sE = sEnt + c0 + half;
            const int last = n - 2;
            int2 en[NS];
            auto read_entries = [&](int i) {
#pragma unroll
                for (int u = 0; u < NS; ++u) en[u] = sE[min(i + 2 * u, last)];
            };
            auto issue_loads = [&](uint4* pv, int* yy) {
#pragma unroll
                for (int u = 0; u < NS; ++u) {
                    yy[u] = en[u].y;
                    pv[u] = *reinterpret_cast<const uint4*>(Pb + ((uint32_t)en[u].x + loff));
                }
            };
            auto compute_slot = [&](const uint4& pv, int yy) {
                int node = __builtin_amdgcn_readfirstlane(yy) >> 16;
                if (node != cur) {
                    if (cur >= 0) flush();
                    float4 qa = *reinterpret_cast<const float4*>(sQ + node * H + sl * 8);
                    float4 qb = *reinterpret_cast<const float4*>(sQ + node * H + sl * 8 + 4);
                    q[0] = f32x2{qa.x, qa.z}; q[1] = f32x2{qa.y, qa.w};
                    q[2] = f32x2{qb.x, qb.z}; q[3] = f32x2{qb.y, qb.w};
                    cur = node;
                }
                float e = __uint_as_float((uint32_t)yy << 16);
                f32x2 ee = {e, e};
                edge4(acc[0], acc[1], pv.x, pv.y, ee, wq[0], wq[1], q[0], q[1]);
                edge4(acc[2], acc[3], pv.z, pv.w, ee, wq[2], wq[3], q[2], q[3]);
            };
            read_entries(0);
            issue_loads(pvA, yyA);
            read_entries(2 * NS);
            for (int i = 0; i < n; i += 4 * NS) {
                issue_loads(pvB, yyB);
                read_entries(i + 4 * NS);
#pragma unroll
                for (int u = 0; u < NS; ++u)
                    if (i + 2 * u < n) compute_slot(pvA[u], yyA[u]);
                issue_loads(pvA, yyA);
                read_entries(i + 6 * NS);
#pragma unroll
                for (int u = 0; u < NS; ++u)
                    if (i + 2 * NS + 2 * u < n) compute_slot(pvB[u], yyB[u]);
            }
            flush();
        }
        if (p0 + CAP >= total) break;
        __syncthreads();
        merge();
        __syncthreads();
        if (tid < MAIN_WAVES * 32) sNcnt[tid] = 0;
        if (tid <= MAIN_WAVES) sCarryNode[tid] = -1;
        __syncthreads();
        stage(p0 + CAP, false);
    }

    bf16x8 afr[8];
#pragma unroll
    for (int ks = 0; ks < 8; ++ks)
        afr[ks] = *reinterpret_cast<const bf16x8*>(w2f + ((size_t)(wv * 8 + ks) * 64 + lane) * 8);
    const uint2 bbv = *reinterpret_cast<const uint2*>(b2 + wv * 16 + (lane >> 4) * 4);
    __syncthreads();
    merge();
    __syncthreads();
    for (int i = tid; i < NPB * (H / 2); i += MAIN_THREADS) {
        const int row = i / (H / 2), c2 = (i % (H / 2)) * 2;
        const float2 sv = *reinterpret_cast<const float2*>(sS + row * SROW + c2);
        const float s0 = sv.x * NEG_LN2, s1 = sv.y * NEG_LN2;
        const __bf16 h0 = (__bf16)s0, h1 = (__bf16)s1;
        const __bf16 l0 = (__bf16)(s0 - (float)h0), l1 = (__bf16)(s1 - (float)h1);
        typedef __attribute__((ext_vector_type(2))) __bf16 bf16x2;
        *reinterpret_cast<bf16x2*>(sHi + row * HROW + c2) = bf16x2{h0, h1};
        *reinterpret_cast<bf16x2*>(sLo + row * HROW + c2) = bf16x2{l0, l1};
    }
    __syncthreads();

    {
        const int cc = wv * 16 + (lane >> 4) * 4;
        float bb[4] = {__uint_as_float(bbv.x << 16), __uint_as_float(bbv.x & 0xffff0000u),
                       __uint_as_float(bbv.y << 16), __uint_as_float(bbv.y & 0xffff0000u)};
#pragma unroll
        for (int nt = 0; nt < 2; ++nt) {
            const int nl = nt * 16 + (lane & 15);
            const int row = min(nl, NPB - 1);
            f32x4 acc = {0.f, 0.f, 0.f, 0.f};
#pragma unroll
            for (int ks = 0; ks < 8; ++ks) {
                bf16x8 hi = *reinterpret_cast<const bf16x8*>(sHi + row * HROW + ks * 32 + (lane >> 4) * 8);
                bf16x8 lo = *reinterpret_cast<const bf16x8*>(sLo + row * HROW + ks * 32 + (lane >> 4) * 8);
                acc = __builtin_amdgcn_mfma_f32_16x16x32_bf16(afr[ks], hi, acc, 0, 0, 0);
                acc = __builtin_amdgcn_mfma_f32_16x16x32_bf16(afr[ks], lo, acc, 0, 0, 0);
            }
            if (nl < nn) {
                float dg = (float)sDeg[nl];
                uint32_t o0 = (uint32_t)f32_to_bf16_bits(acc[0] + dg * bb[0]) | ((uint32_t)f32_to_bf16_bits(acc[1] + dg * bb[1]) << 16);
                uint32_t o1 = (uint32_t)f32_to_bf16_bits(acc[2] + dg * bb[2]) | ((uint32_t)f32_to_bf16_bits(acc[3] + dg * bb[3]) << 16);
                uint2 v; v.x = o0; v.y = o1;
                *reinterpret_cast<uint2*>(out + (size_t)(n0 + nl) * D + cc) = v;
            }
        }
    }
}

static inline size_t align_up(size_t v, size_t a) { return (v + a - 1) / a * a; }

extern "C" void kernel_launch(void* const* d_in, const int* in_sizes, int n_in,
                              void* d_out, int out_size, void* d_ws, size_t ws_size,
                              hipStream_t stream) {
    const uint16_t* x = (const uint16_t*)d_in[0];
    const uint16_t* e_attr = (const uint16_t*)d_in[1];
    const int* src = (const int*)d_in[2];
    const int* dst = (const int*)d_in[3];
    const uint16_t* w1 = (const uint16_t*)d_in[4];
    const uint16_t* b1 = (const uint16_t*)d_in[5];
    const uint16_t* w2 = (const uint16_t*)d_in[6];
    const uint16_t* b2 = (const uint16_t*)d_in[7];
    uint16_t* out = (uint16_t*)d_out;

    const int N = in_sizes[0] / D;
    const int E = in_sizes[2];
    const int NB = (((N + NPB - 1) / NPB + 511) / 512) * 512;
    const int chunk_len = (E + NCHUNK - 1) / NCHUNK;
    const int n_gemm_blocks = ((N + 127) / 128) * 4;

    unsigned char* ws = (unsigned char*)d_ws;
    size_t off = 0;
    uint16_t* P = (uint16_t*)(ws + off); off = align_up(off + (size_t)(N + 1) * H * 2, 256);
    float* Q = (float*)(ws + off); off = align_up(off + (size_t)N * H * 4, 256);
    uint16_t* w2f = (uint16_t*)(ws + off); off = align_up(off + (size_t)H * D * 2, 256);
    int2* ent = (int2*)(ws + off); off = align_up(off + (size_t)NCHUNK * chunk_len * 8, 256);
    int2* offs = (int2*)(ws + off); off = align_up(off + (size_t)NCHUNK * NB * 8, 256);

    prep_kernel<<<NCHUNK + n_gemm_blocks + 32, PRE_THREADS, 0, stream>>>(
        x, e_attr, src, dst, w1, b1, w2, P, Q, w2f, ent, offs, N, E, NB, chunk_len, n_gemm_blocks);
    main_kernel<<<NB, MAIN_THREADS, 0, stream>>>(P, Q, w1, w2f, b2, ent, offs, out, N, NB, chunk_len);
}
```
